```python
import torch

def _softmax(x, axis=-1):
    return torch.softmax(x, dim=axis)
def _gelu(x):
    return torch.nn.functional.gelu(x, approximate="tanh")
def _silu(x):
    return torch.nn.functional.silu(x)
def _sigmoid(x):
    return torch.sigmoid(x)
def _layernorm(x, axis=-1, eps=1e-5):
    m = x.mean(dim=axis, keepdim=True)
    v = x.var(dim=axis, keepdim=True, unbiased=False)
    return (x - m) / torch.sqrt(v + eps)
def _scatter_add(tgt, idx, src, axis=0):
    return tgt.clone().index_add_(axis, idx, src)
def _segment_sum(src, seg, n):
    out = torch.zeros((n,) + src.shape[1:], dtype=src.dtype)
    return out.index_add_(0, seg, src)
def _pad(x, pw):
    flat = tuple(v for pair in reversed(pw) for v in pair)
    return torch.nn.functional.pad(x, flat)

kernel_name = 'gated_mix__sliding_block__dense_activation_mish__prompt'

def setup_inputs():

    _w = (lambda a: (lambda t: t.to(torch.bfloat16) if t.is_floating_point() else t)(torch.as_tensor(a)))
    def _A_setup():
        B, S, D = 1, 2048, 512
        return {
            "x": _w((lambda *s: torch.randn(*s, dtype=torch.bfloat16) * 0.05)(B, S, D)),
            "wqkv": _w((lambda *s: torch.randn(*s, dtype=torch.bfloat16) * 0.8)(D, 3 * D)),
            "wo": _w((lambda *s: torch.randn(*s, dtype=torch.bfloat16) * 0.05)(D, D)),
        }
    _A_in = _A_setup()

    def _B_setup():
        B, S, D, F = 1, 2048, 512, 1376
        return {
            "x": _w((lambda *s: torch.randn(*s, dtype=torch.bfloat16) * 0.05)(B, S, D)),
            "w1": _w((lambda *s: torch.randn(*s, dtype=torch.bfloat16) * 12.8)(D, F) * (D ** -0.5)),
            "b1": _w((lambda *s: torch.randn(*s, dtype=torch.bfloat16) * 0.8)(F)),
            "w2": _w((lambda *s: torch.randn(*s, dtype=torch.bfloat16) * 0.8)(F, D) * (F ** -0.5)),
        }
    _B_in = _B_setup()

    return {
        "A_x": _A_in["x"],
        "A_wqkv": _A_in["wqkv"],
        "A_wo": _A_in["wo"],
        "B_x": _B_in["x"],
        "B_w1": _B_in["w1"],
        "B_b1": _B_in["b1"],
        "B_w2": _B_in["w2"],
        "gate_w": _w((lambda *s: torch.randn(*s, dtype=torch.bfloat16) * 0.05)(512) * 16.0),
    }

def reference(A_x, A_wqkv, A_wo, B_x, B_w1, B_b1, B_w2, gate_w):
    def _A_ref(x, wqkv, wo):
        B, S, D = x.shape
        H = 32; Dh = D // H; W = 256
        qkv = (lambda x, *d: x.permute(*d))((x @ wqkv).reshape(B, S, 3, H, Dh), 2, 0, 3, 1, 4)
        q, k, v = qkv[0], qkv[1], qkv[2]
        scores = torch.einsum("bhqd,bhkd->bhqk", q, k) * (Dh ** -0.5)
        idx = torch.arange(S)
        mask = (idx[:, None] - idx[None, :] >= 0) & (idx[:, None] - idx[None, :] < W)
        scores = torch.where(mask[None, None], scores, -1e9)
        p = _softmax(scores, axis=-1)
        o = torch.einsum("bhqk,bhkd->bhqd", p, v)
        return (lambda x, *d: x.permute(*d))(o, 0, 2, 1, 3).reshape(B, S, D) @ wo

    def _B_ref(x, w1, b1, w2):
        h = torch.einsum("bsd,df->bsf", x, w1) + b1
        sp = torch.log1p(torch.exp(-torch.abs(h))) + torch.where(h > 0, h, torch.zeros_like(h))
        act = h * torch.tanh(sp)
        return torch.einsum("bsf,fd->bsd", act, w2)

    x = A_x
    g = _sigmoid((x * gate_w).sum(-1, keepdims=True))
    a = _A_ref(x, A_wqkv, A_wo)
    b = _B_ref(x, B_w1, B_b1, B_w2)
    return g * a + (1 - g) * b
```

```cpp
#include <hip/hip_runtime.h>
#include <hip/hip_bf16.h>

#define LAS __attribute__((address_space(3)))
#define GAS __attribute__((address_space(1)))
typedef __attribute__((ext_vector_type(8))) __bf16 bf16x8;
typedef __attribute__((ext_vector_type(4))) __bf16 bf16x4;
typedef __attribute__((ext_vector_type(16))) float f32x16;
typedef __attribute__((ext_vector_type(4))) float f32x4;
typedef __attribute__((ext_vector_type(4))) unsigned u32x4;
typedef __attribute__((ext_vector_type(2))) unsigned u32x2;

constexpr int S_ = 2048, D_ = 512, F_ = 1376;
constexpr int KS2 = F_ / 16;
constexpr int NFRAG_W2 = KS2 * 16;
constexpr int NFRAG_W = NFRAG_W2 + 32 * 16;
constexpr size_t OFF_QKV = 0;
constexpr size_t OFF_ACT = OFF_QKV + (size_t)96 * 2048 * 32;
constexpr size_t OFF_WF = OFF_ACT + (size_t)64 * KS2 * 1024;
constexpr size_t OFF_O = OFF_WF + (size_t)NFRAG_W * 1024;
constexpr size_t OFF_G = OFF_O + (size_t)64 * 32 * 1024;
constexpr size_t OFF_CNT = OFF_G + 2048 * 4;

#define MFMA32(a, b, c) __builtin_amdgcn_mfma_f32_32x32x16_bf16((a), (b), (c), 0, 0, 0)
#define WAIT_V(n) asm volatile("s_waitcnt vmcnt(" #n ")" ::: "memory")
#define BARRIER() do { __builtin_amdgcn_s_barrier(); asm volatile("" ::: "memory"); } while (0)
#define GLDS16(g, l) __builtin_amdgcn_global_load_lds((const GAS void*)(g), (LAS void*)(l), 16, 0, 0)

__device__ __forceinline__ float bf2f(unsigned short v) { return __uint_as_float(((unsigned)v) << 16); }
__device__ __forceinline__ bf16x8 pack8(const float* v) {
  bf16x8 r;
#pragma unroll
  for (int j = 0; j < 8; ++j) r[j] = (__bf16)v[j];
  return r;
}

__global__ void __launch_bounds__(512, 2)
k1_proj(const unsigned short* __restrict__ x, const unsigned short* __restrict__ wqkv, const unsigned short* __restrict__ wo,
        const unsigned short* __restrict__ w1, const unsigned short* __restrict__ b1, const unsigned short* __restrict__ w2,
        const unsigned short* __restrict__ gate_w, char* __restrict__ ws) {
  __shared__ __attribute__((aligned(1024))) char lds[4 * 40960];
  const int tid = threadIdx.x, lane = tid & 63;
  const int w = __builtin_amdgcn_readfirstlane(tid >> 6);
  const int b = blockIdx.x;
  const int xcd = b & 7, idx = b >> 3;
  const int rt = (xcd & 1) * 8 + (idx & 7);
  const int ct = (xcd >> 1) * 4 + (idx >> 3);
  const int h = lane >> 5, l31 = lane & 31;
  const int row0 = rt * 128;

  unsigned short rt_v[8];
  const int fid = b * 8 + w;
  {
    const int fc = fid < NFRAG_W ? fid : 0;
    const bool isw2 = fc < NFRAG_W2;
    const int f2 = isw2 ? fc : fc - NFRAG_W2;
    const unsigned short* src = isw2 ? w2 : wo;
    const int s = f2 >> 4, c = f2 & 15;
#pragma unroll
    for (int j = 0; j < 8; ++j) {
      const int k = 16 * s + (isw2 ? ((j & 3) + 8 * (j >> 2) + 4 * h) : (8 * h + j));
      rt_v[j] = src[k * 512 + 32 * c + l31];
    }
  }
  const int gtok = row0 + ct * 8 + w;
  const u32x4 gx = *(const u32x4*)(x + gtok * 512 + lane * 8);
  const u32x4 gw = *(const u32x4*)(gate_w + lane * 8);

  const bool isqkv = ct < 8;
  const unsigned short* wsrc = isqkv ? wqkv : w1;
  const int ldw = isqkv ? 1536 : F_;
  const int col0 = isqkv ? 192 * ct : 192 * (ct - 8);
  const unsigned short* xg[2];
#pragma unroll
  for (int p = 0; p < 2; ++p) {
    const int r = (2 * w + p) * 8 + (lane >> 3);
    const int ch = (lane & 7) ^ ((r >> 1) & 7);
    xg[p] = x + (row0 + r) * 512 + ch * 8;
  }
  const unsigned short* wg[3];
#pragma unroll
  for (int cp = 0; cp < 3; ++cp) {
    const int k = 8 * w + (lane >> 3);
    const int cc = lane & 7;
    const int half = (cc >> 2) ^ ((lane >> 4) & 1);
    int col = col0 + 64 * cp + 32 * half + 8 * (cc & 3);
    col = col < ldw - 8 ? col : ldw - 8;
    wg[cp] = wsrc + k * ldw + col;
  }
  const int wstep = 64 * ldw;
  auto issue = [&](int stage) {
    char* base = lds + (stage & 3) * 40960;
#pragma unroll
    for (int p = 0; p < 2; ++p) GLDS16(xg[p] + stage * 64, base + (2 * w + p) * 1024);
#pragma unroll
    for (int cp = 0; cp < 3; ++cp) GLDS16(wg[cp] + stage * wstep, base + 16384 + cp * 8192 + w * 1024);
  };
  issue(0); issue(1); issue(2); issue(3);

  const int wr = w & 3, wc = w >> 2;
  f32x16 acc[3];
#pragma unroll
  for (int i = 0; i < 3; ++i)
#pragma unroll
    for (int r = 0; r < 16; ++r) acc[i][r] = 0.f;

  const unsigned ldsbase = (unsigned)(size_t)(LAS char*)lds;
  const int xrow = 32 * wr + l31;
  const int xsw = (xrow >> 1) & 7;
  int xoff[4];
#pragma unroll
  for (int s = 0; s < 4; ++s) xoff[s] = xrow * 128 + (((2 * s + h) ^ xsw) << 4);
  int woff[3];
  {
    const int blk = (lane >> 4) & 1, q = (lane & 15) >> 2, p = lane & 3;
#pragma unroll
    for (int i = 0; i < 3; ++i) {
      const int st = 3 * wc + i, cp = st >> 1, half = st & 1;
      woff[i] = 16384 + cp * 8192 + (8 * h + q) * 128 + ((half ^ ((q >> 1) & 1)) << 6) + 32 * blk + 8 * p;
    }
  }

#pragma unroll
  for (int it = 0; it < 8; ++it) {
    if (it == 0) WAIT_V(15);
    else if (it <= 5) WAIT_V(10);
    else if (it == 6) WAIT_V(5);
    else WAIT_V(0);
    BARRIER();
    if (it >= 1 && it + 3 < 8) issue(it + 3);
    const unsigned sb = ldsbase + (it & 3) * 40960;
    bf16x8 xf[2];
    bf16x4 wl[2][3], wh[2][3];
    auto rd = [&](int s, int buf) {
      asm volatile("ds_read_b128 %0, %1" : "=v"(xf[buf]) : "v"(sb + xoff[s]) : "memory");
#pragma unroll
      for (int i = 0; i < 3; ++i) {
        asm volatile("ds_read_b64_tr_b16 %0, %1 offset:%2" : "=v"(wl[buf][i]) : "v"(sb + woff[i]), "n"(s * 2048) : "memory");
        asm volatile("ds_read_b64_tr_b16 %0, %1 offset:%2" : "=v"(wh[buf][i]) : "v"(sb + woff[i]), "n"(s * 2048 + 512) : "memory");
      }
    };
    rd(0, 0);
    rd(1, 1);
#pragma unroll
    for (int s = 0; s < 4; ++s) {
      const int buf = s & 1;
      if (s < 3)
        asm volatile("s_waitcnt lgkmcnt(7)" : "+v"(xf[buf]), "+v"(wl[buf][0]), "+v"(wh[buf][0]), "+v"(wl[buf][1]), "+v"(wh[buf][1]), "+v"(wl[buf][2]), "+v"(wh[buf][2]) :: "memory");
      else
        asm volatile("s_waitcnt lgkmcnt(0)" : "+v"(xf[buf]), "+v"(wl[buf][0]), "+v"(wh[buf][0]), "+v"(wl[buf][1]), "+v"(wh[buf][1]), "+v"(wl[buf][2]), "+v"(wh[buf][2]) :: "memory");
#pragma unroll
      for (int i = 0; i < 3; ++i) {
        const bf16x8 wf = __builtin_shufflevector(wl[buf][i], wh[buf][i], 0, 1, 2, 3, 4, 5, 6, 7);
        acc[i] = MFMA32(wf, xf[buf], acc[i]);
      }
      if (s + 2 < 4) rd(s + 2, buf);
    }
  }

  const int tok = row0 + 32 * wr + l31;
  if (isqkv) {
#pragma unroll
    for (int i = 0; i < 3; ++i) {
      const int gst = 6 * ct + 3 * wc + i;
      const float sc = gst < 16 ? 0.25f * 1.4426950408889634f : 1.0f;
#pragma unroll
      for (int e = 0; e < 2; ++e) {
        float v[8];
#pragma unroll
        for (int j = 0; j < 8; ++j) v[j] = acc[i][8 * e + j] * sc;
        *(bf16x8*)(ws + OFF_QKV + ((size_t)(2 * gst + e) * 2048 + tok) * 32 + h * 16) = pack8(v);
      }
    }
  } else {
#pragma unroll
    for (int i = 0; i < 3; ++i) {
      const int hs = 6 * (ct - 8) + 3 * wc + i;
      if (hs < 43) {
#pragma unroll
        for (int e = 0; e < 2; ++e) {
          float v[8];
#pragma unroll
          for (int g2 = 0; g2 < 2; ++g2) {
            const u32x2 bb = *(const u32x2*)(b1 + 32 * hs + 16 * e + 8 * g2 + 4 * h);
            const float bv[4] = {__uint_as_float(bb[0] << 16), __uint_as_float(bb[0] & 0xffff0000u),
                                 __uint_as_float(bb[1] << 16), __uint_as_float(bb[1] & 0xffff0000u)};
#pragma unroll
            for (int j = 0; j < 4; ++j) {
              const float hv = acc[i][8 * e + 4 * g2 + j] + bv[j];
              const float ex = __builtin_amdgcn_exp2f(fminf(hv, 20.f) * 1.4426950408889634f);
              const float t = ex * (ex + 2.f);
              v[4 * g2 + j] = hv * t * __builtin_amdgcn_rcpf(t + 2.f);
            }
          }
          *(bf16x8*)(ws + OFF_ACT + ((size_t)((rt * 4 + wr) * KS2 + 2 * hs + e) * 64 + lane) * 16) = pack8(v);
        }
      }
    }
  }
  if (fid < NFRAG_W) {
    u32x4 o;
#pragma unroll
    for (int j = 0; j < 4; ++j) o[j] = (unsigned)rt_v[2 * j] | ((unsigned)rt_v[2 * j + 1] << 16);
    *(u32x4*)(ws + OFF_WF + (size_t)fid * 1024 + lane * 16) = o;
  }
  {
    float s = 0.f;
#pragma unroll
    for (int j = 0; j < 4; ++j) {
      s += __uint_as_float(gx[j] << 16) * __uint_as_float(gw[j] << 16);
      s += __uint_as_float(gx[j] & 0xffff0000u) * __uint_as_float(gw[j] & 0xffff0000u);
    }
#pragma unroll
    for (int o = 32; o >= 1; o >>= 1) s += __shfl_xor(s, o);
    if (lane == 0) ((float*)(ws + OFF_G))[gtok] = __builtin_amdgcn_rcpf(1.f + __builtin_amdgcn_exp2f(-s * 1.4426950408889634f));
  }
  if (b == 0 && tid < 32) *(unsigned*)(ws + OFF_CNT + tid * 256) = 0u;
}

constexpr int RED_STRIDE = 272;
constexpr int RED_TILE = 64 * RED_STRIDE;
constexpr int K2_LDS = 4 * RED_TILE + 256;

__global__ void __launch_bounds__(512, 2)
k2_attn_mlp(char* __restrict__ ws, unsigned short* __restrict__ out) {
  __shared__ __attribute__((aligned(1024))) char lds[K2_LDS];
  const int tid = threadIdx.x, lane = tid & 63;
  const int w = __builtin_amdgcn_readfirstlane(tid >> 6);
  const int b = blockIdx.x;
  const int xcd = b & 7, idx = b >> 3;
  const int rt = xcd * 4 + (idx >> 3);
  const int jt = idx & 7;
  const int h = lane >> 5, l31 = lane & 31;
  unsigned* cnt = (unsigned*)(ws + OFF_CNT + rt * 256);
  unsigned* lds_words = (unsigned*)(lds + 4 * RED_TILE);
  const __amdgpu_buffer_rsrc_t orsrc = __builtin_amdgcn_make_buffer_rsrc(ws + OFF_O, 0, 64 * 32 * 1024, 0x00020000);
  const float* gate = (const float*)(ws + OFF_G);

  if (tid < 17) lds_words[tid] = tid < 16 ? 0x3F803F80u : 0u;
  __syncthreads();

  f32x16 acc[2][2];
  if (w >= 4) {
    const int head = 4 * jt + (w - 4);
    char* vl = lds + (w - 4) * 10240;
    const char* qb = ws + OFF_QKV + (size_t)head * 2048 * 32;
    const char* kb = qb + (size_t)32 * 2048 * 32;
    const char* vb = kb + (size_t)32 * 2048 * 32;
    const int blk0 = 2 * rt - 8;
    bf16x8 kf[10], qf[2];
#pragma unroll
    for (int i = 0; i < 10; ++i) {
      const int gb = blk0 + i < 0 ? 0 : blk0 + i;
      GLDS16(vb + gb * 1024 + lane * 16, vl + i * 1024);
    }
#pragma unroll
    for (int u = 0; u < 2; ++u) qf[u] = *(const bf16x8*)(qb + (2 * rt + u) * 1024 + l31 * 32 + h * 16);
#pragma unroll
    for (int i = 0; i < 10; ++i) {
      const int gb = blk0 + i < 0 ? 0 : blk0 + i;
      kf[i] = *(const bf16x8*)(kb + gb * 1024 + l31 * 32 + h * 16);
    }
    float gq[2];
#pragma unroll
    for (int u = 0; u < 2; ++u) gq[u] = gate[64 * rt + 32 * u + l31];
    __builtin_amdgcn_s_waitcnt(0x0F70);
    const int tq = (lane & 15) >> 2, tp = lane & 3;
    const bool onesl = (lane >> 4) & 1;
    const int vaddr = onesl ? (4 * RED_TILE + 8 * tp) : ((w - 4) * 10240 + (4 * h + tq) * 32 + 8 * tp);
    const int vmul = onesl ? 0 : 1;
    const int z = l31 - 4 * h;
#pragma unroll
    for (int u = 0; u < 2; ++u) {
      float m = -3.0e38f;
      f32x16 zero;
#pragma unroll
      for (int r = 0; r < 16; ++r) zero[r] = 0.f;
#pragma unroll
      for (int i = 0; i < 9; ++i) {
        if (blk0 + u + i >= 0) {
          f32x16 s = MFMA32(kf[u + i], qf[u], zero);
          if (i == 0) {
#pragma unroll
            for (int r = 0; r < 16; ++r) s[r] = ((r & 3) + 8 * (r >> 2) > z) ? s[r] : -3.0e38f;
          }
          if (i == 8) {
#pragma unroll
            for (int r = 0; r < 16; ++r) s[r] = ((r & 3) + 8 * (r >> 2) <= z) ? s[r] : -3.0e38f;
          }
#pragma unroll
          for (int r = 0; r < 16; r += 2) m = fmaxf(fmaxf(m, s[r]), s[r + 1]);
        }
      }
      m = fmaxf(m, __shfl_xor(m, 32));
      f32x16 negm, o;
#pragma unroll
      for (int r = 0; r < 16; ++r) { negm[r] = -m; o[r] = 0.f; }
#pragma unroll
      for (int i = 0; i < 9; ++i) {
        if (blk0 + u + i >= 0) {
          f32x16 s = MFMA32(kf[u + i], qf[u], negm);
          float p[16];
#pragma unroll
          for (int r = 0; r < 16; ++r) p[r] = __builtin_amdgcn_exp2f(s[r]);
          if (i == 0) {
#pragma unroll
            for (int r = 0; r < 16; ++r) p[r] = ((r & 3) + 8 * (r >> 2) > z) ? p[r] : 0.f;
          }
          if (i == 8) {
#pragma unroll
            for (int r = 0; r < 16; ++r) p[r] = ((r & 3) + 8 * (r >> 2) <= z) ? p[r] : 0.f;
          }
#pragma unroll
          for (int e = 0; e < 2; ++e) {
            const int a0 = vaddr + vmul * ((u + i) * 1024 + e * 512);
            const bf16x4 lo = __builtin_amdgcn_ds_read_tr16_b64_v4bf16((LAS bf16x4*)(lds + a0));
            const bf16x4 hi = __builtin_amdgcn_ds_read_tr16_b64_v4bf16((LAS bf16x4*)(lds + a0 + vmul * 256));
            const bf16x8 vf = __builtin_shufflevector(lo, hi, 0, 1, 2, 3, 4, 5, 6, 7);
            o = MFMA32(vf, pack8(p + 8 * e), o);
          }
        }
      }
      const float inv = gq[u] * __builtin_amdgcn_rcpf(o[8]);
      float ov[8];
#pragma unroll
      for (int j = 0; j < 8; ++j) ov[j] = o[j] * inv;
      const bf16x8 ob = pack8(ov);
      __builtin_amdgcn_raw_buffer_store_b128(__builtin_bit_cast(u32x4, ob), orsrc, (((2 * rt + u) * 32 + head) * 64 + lane) * 16, 0, 16);
    }
    WAIT_V(0);
    if (lane == 0) {
      const unsigned old = __hip_atomic_fetch_add((LAS unsigned*)(lds_words + 16), 1u, __ATOMIC_RELAXED, __HIP_MEMORY_SCOPE_WORKGROUP);
      if (old == 3u) __hip_atomic_fetch_add(cnt, 1u, __ATOMIC_RELAXED, __HIP_MEMORY_SCOPE_AGENT);
    }
    if (w == 4) {
      if (lane == 0) {
        unsigned spins = 0;
        while (__hip_atomic_load(cnt, __ATOMIC_RELAXED, __HIP_MEMORY_SCOPE_AGENT) < 8u) {
          __builtin_amdgcn_s_sleep(2);
          if (++spins > (1u << 22)) break;
        }
      }
    }
    BARRIER();
  } else {
    const char* af = ws + OFF_ACT + (size_t)(2 * rt) * KS2 * 1024 + lane * 16;
    const char* wf = ws + OFF_WF + (size_t)(2 * jt) * 1024 + lane * 16;
    const int s0 = w < 2 ? 22 * w : 44 + 21 * (w - 2);
    const int ns = w < 2 ? 22 : 21;
    constexpr int RD = 8;
    bf16x8 ra[RD][2], rb[RD][2];
#pragma unroll
    for (int c = 0; c < 2; ++c)
#pragma unroll
      for (int t = 0; t < 2; ++t)
#pragma unroll
        for (int r = 0; r < 16; ++r) acc[c][t][r] = 0.f;
#pragma unroll
    for (int i = 0; i < RD; ++i) {
      const int s = s0 + i;
      ra[i][0] = *(const bf16x8*)(wf + (size_t)s * 16384);
      ra[i][1] = *(const bf16x8*)(wf + (size_t)s * 16384 + 1024);
      rb[i][0] = *(const bf16x8*)(af + s * 1024);
      rb[i][1] = *(const bf16x8*)(af + s * 1024 + KS2 * 1024);
    }
    const char* wof = ws + OFF_WF + (size_t)(NFRAG_W2 + 2 * jt) * 1024 + lane * 16;
#pragma unroll
    for (int i = 0; i < 22; ++i) {
      const int sl = i % RD;
      if (i < 21 || ns == 22) {
#pragma unroll
        for (int c = 0; c < 2; ++c)
#pragma unroll
          for (int t = 0; t < 2; ++t) acc[c][t] = MFMA32(ra[sl][c], rb[sl][t], acc[c][t]);
      }
      if (i + RD < 22) {
        int s = s0 + i + RD;
        s = s < KS2 ? s : KS2 - 1;
        ra[sl][0] = *(const bf16x8*)(wf + (size_t)s * 16384);
        ra[sl][1] = *(const bf16x8*)(wf + (size_t)s * 16384 + 1024);
        rb[sl][0] = *(const bf16x8*)(af + s * 1024);
        rb[sl][1] = *(const bf16x8*)(af + s * 1024 + KS2 * 1024);
      } else {
        const int hh = 8 * w + (i + RD - 22);
        ra[sl][0] = *(const bf16x8*)(wof + (size_t)hh * 16384);
        ra[sl][1] = *(const bf16x8*)(wof + (size_t)hh * 16384 + 1024);
      }
      __builtin_amdgcn_sched_barrier(0);
    }
#pragma unroll
    for (int t = 0; t < 2; ++t) {
      const float og = 1.f - gate[64 * rt + 32 * t + l31];
#pragma unroll
      for (int c = 0; c < 2; ++c)
#pragma unroll
        for (int r = 0; r < 16; ++r) acc[c][t][r] *= og;
    }
    BARRIER();
#pragma unroll
    for (int i = 0; i < 8; ++i) {
      const int sl = (14 + i) % RD;
#pragma unroll
      for (int t = 0; t < 2; ++t)
        rb[sl][t] = __builtin_bit_cast(bf16x8, __builtin_amdgcn_raw_buffer_load_b128(orsrc, (((2 * rt + t) * 32 + 8 * w + i) * 64 + lane) * 16, 0, 16));
    }
#pragma unroll
    for (int i = 0; i < 8; ++i) {
      const int sl = (14 + i) % RD;
#pragma unroll
      for (int c = 0; c < 2; ++c)
#pragma unroll
        for (int t = 0; t < 2; ++t) acc[c][t] = MFMA32(ra[sl][c], rb[sl][t], acc[c][t]);
    }
    char* red = lds + w * RED_TILE;
#pragma unroll
    for (int c = 0; c < 2; ++c)
#pragma unroll
      for (int t = 0; t < 2; ++t)
#pragma unroll
        for (int g = 0; g < 4; ++g) {
          f32x4 v = {acc[c][t][4 * g], acc[c][t][4 * g + 1], acc[c][t][4 * g + 2], acc[c][t][4 * g + 3]};
          *(f32x4*)(red + (32 * t + l31) * RED_STRIDE + (32 * c + 8 * g + 4 * h) * 4) = v;
        }
  }
  __syncthreads();
  {
    const int tok = tid >> 3, c8 = tid & 7;
    float v[8];
#pragma unroll
    for (int j = 0; j < 8; ++j) v[j] = 0.f;
#pragma unroll
    for (int p = 0; p < 4; ++p) {
      const f32x4 a = *(const f32x4*)(lds + p * RED_TILE + tok * RED_STRIDE + c8 * 32);
      const f32x4 c = *(const f32x4*)(lds + p * RED_TILE + tok * RED_STRIDE + c8 * 32 + 16);
#pragma unroll
      for (int j = 0; j < 4; ++j) { v[j] += a[j]; v[4 + j] += c[j]; }
    }
    *(bf16x8*)(out + (size_t)(64 * rt + tok) * 512 + 64 * jt + c8 * 8) = pack8(v);
  }
}

extern "C" void kernel_launch(void* const* d_in, const int* in_sizes, int n_in, void* d_out, int out_size, void* d_ws,
                              size_t ws_size, hipStream_t stream) {
  const unsigned short* x = (const unsigned short*)d_in[0];
  const unsigned short* wqkv = (const unsigned short*)d_in[1];
  const unsigned short* wo = (const unsigned short*)d_in[2];
  const unsigned short* w1 = (const unsigned short*)d_in[4];
  const unsigned short* b1 = (const unsigned short*)d_in[5];
  const unsigned short* w2 = (const unsigned short*)d_in[6];
  const unsigned short* gw = (const unsigned short*)d_in[7];
  k1_proj<<<256, 512, 0, stream>>>(x, wqkv, wo, w1, b1, w2, gw, (char*)d_ws);
  k2_attn_mlp<<<256, 512, 0, stream>>>((char*)d_ws, (unsigned short*)d_out);
}
```

```cpp
#include <hip/hip_runtime.h>
#include <hip/hip_bf16.h>
#include <type_traits>

#define LAS __attribute__((address_space(3)))
#define GAS __attribute__((address_space(1)))
typedef __attribute__((ext_vector_type(8))) __bf16 bf16x8;
typedef __attribute__((ext_vector_type(4))) __bf16 bf16x4;
typedef __attribute__((ext_vector_type(16))) float f32x16;
typedef __attribute__((ext_vector_type(4))) float f32x4;
typedef __attribute__((ext_vector_type(4))) unsigned u32x4;
typedef __attribute__((ext_vector_type(2))) unsigned u32x2;

constexpr int S_ = 2048, D_ = 512, F_ = 1376;
constexpr int KS2 = F_ / 16;
constexpr int NFRAG_W2 = KS2 * 16;
constexpr int NFRAG_W = NFRAG_W2 + 32 * 16;
constexpr size_t OFF_QKV = 0;
constexpr size_t OFF_ACT = OFF_QKV + (size_t)96 * 2048 * 32;
constexpr size_t OFF_WF = OFF_ACT + (size_t)64 * KS2 * 1024;
constexpr size_t OFF_O = OFF_WF + (size_t)NFRAG_W * 1024;
constexpr size_t OFF_G = OFF_O + (size_t)64 * 32 * 1024;
constexpr size_t OFF_CNT = OFF_G + 2048 * 4;

constexpr size_t OFF_STAMP = OFF_CNT + 32 * 256;
#define STAMP(kk, i) do {} while (0)
#define CSTAMP(kk, i) do {} while (0)
#define ROT (rt + ct)
#define MFMA32(a, b, c) __builtin_amdgcn_mfma_f32_32x32x16_bf16((a), (b), (c), 0, 0, 0)
#define WAIT_V(n) asm volatile("s_waitcnt vmcnt(" #n ")" ::: "memory")
#define BARRIER() do { __builtin_amdgcn_s_barrier(); asm volatile("" ::: "memory"); } while (0)
#define GLDS16(g, l) __builtin_amdgcn_global_load_lds((const GAS void*)(g), (LAS void*)(l), 16, 0, 0)

__device__ __forceinline__ float bf2f(unsigned short v) { return __uint_as_float(((unsigned)v) << 16); }
__device__ __forceinline__ bf16x8 pack8(const float* v) {
  bf16x8 r;
#pragma unroll
  for (int j = 0; j < 8; ++j) r[j] = (__bf16)v[j];
  return r;
}

__global__ void __launch_bounds__(512, 2)
k1_proj(const unsigned short* __restrict__ x, const unsigned short* __restrict__ wqkv, const unsigned short* __restrict__ wo,
        const unsigned short* __restrict__ w1, const unsigned short* __restrict__ b1, const unsigned short* __restrict__ w2,
        const unsigned short* __restrict__ gate_w, char* __restrict__ ws) {
  __shared__ __attribute__((aligned(1024))) char lds[4 * 40960];
  const int tid = threadIdx.x, lane = tid & 63;
  const int w = __builtin_amdgcn_readfirstlane(tid >> 6);
  const int b = blockIdx.x;
  const int xcd = b & 7, idx = b >> 3;
  const int rt = (xcd & 1) * 8 + (idx & 7);
  const int ct = (xcd >> 1) * 4 + (idx >> 3);
  const int h = lane >> 5, l31 = lane & 31;
  const int row0 = rt * 128;

  STAMP(0, 0); CSTAMP(0, 6);
  asm volatile("" ::: "memory");
  unsigned short rt_v[8];
  const int fid = b * 8 + w;
  const int gtok = row0 + ct * 8 + w;
  u32x4 gx, gw;
  const int m3 = 3 * (ct >> 1), odd = ct & 1;
  int pidx[3];
  bool pkind[3];
  pidx[0] = odd ? m3 + 2 : m3;      pkind[0] = false;
  pidx[1] = m3 + 1;                 pkind[1] = odd;
  pidx[2] = odd ? m3 + 2 : m3;      pkind[2] = true;
  const unsigned short* xg[2];
#pragma unroll
  for (int p = 0; p < 2; ++p) {
    const int r = (2 * w + p) * 8 + (lane >> 3);
    const int ch = (lane & 7) ^ ((r >> 1) & 7);
    xg[p] = x + (row0 + r) * 512 + ch * 8;
  }
  const unsigned short* wg[3];
  int wstep[3];
#pragma unroll
  for (int cp = 0; cp < 3; ++cp) {
    const int ldw = pkind[cp] ? F_ : 1536;
    const int k = 8 * w + (lane >> 3);
    const int cc = lane & 7;
    const int half = (cc >> 2) ^ ((lane >> 4) & 1);
    int col = 64 * pidx[cp] + 32 * half + 8 * (cc & 3);
    col = col < ldw - 8 ? col : ldw - 8;
    wg[cp] = (pkind[cp] ? w1 : wqkv) + k * ldw + col;
    wstep[cp] = 64 * ldw;
  }
  auto issue = [&](int stage) {
    char* base = lds + (stage & 3) * 40960;
    const int ks = (stage + ROT) & 7;
#pragma unroll
    for (int p = 0; p < 2; ++p) GLDS16(xg[p] + ks * 64, base + (2 * w + p) * 1024);
#pragma unroll
    for (int cp = 0; cp < 3; ++cp) GLDS16(wg[cp] + ks * wstep[cp], base + 16384 + cp * 8192 + w * 1024);
  };
  issue(0); issue(1); issue(2); issue(3);
  asm volatile("" ::: "memory");
  const int wr = w & 3, wc = w >> 2;
  u32x2 bb[2][4];
  f32x16 acc[3];
#pragma unroll
  for (int i = 0; i < 3; ++i)
#pragma unroll
    for (int r = 0; r < 16; ++r) acc[i][r] = 0.f;
  const unsigned ldsbase = (unsigned)(size_t)(LAS char*)lds;
  unsigned xoff[4];
  {
    const int xrow = 32 * wr + l31;
    const int xsw = (xrow >> 1) & 7;
#pragma unroll
    for (int s = 0; s < 4; ++s) xoff[s] = ldsbase + xrow * 128 + (((2 * s + h) ^ xsw) << 4);
  }
  unsigned woff[3];
  {
    const int blk = (lane >> 4) & 1, q = (lane & 15) >> 2, p = lane & 3;
#pragma unroll
    for (int i = 0; i < 3; ++i)
      woff[i] = ldsbase + 16384 + i * 8192 + (8 * h + q) * 128 + ((wc ^ ((q >> 1) & 1)) << 6) + 32 * blk + 8 * p;
  }
  bf16x8 xf[2];
  bf16x4 wl[2][3], wh[2][3];
  auto rd = [&](int it, int s, int buf) {
    const unsigned so = (it & 3) * 40960;
    asm volatile("ds_read_b128 %0, %1" : "=v"(xf[buf]) : "v"(xoff[s] + so) : "memory");
#pragma unroll
    for (int i = 0; i < 3; ++i) {
      asm volatile("ds_read_b64_tr_b16 %0, %1 offset:%2" : "=v"(wl[buf][i]) : "v"(woff[i] + so), "n"(s * 2048) : "memory");
      asm volatile("ds_read_b64_tr_b16 %0, %1 offset:%2" : "=v"(wh[buf][i]) : "v"(woff[i] + so), "n"(s * 2048 + 512) : "memory");
    }
  };
#define K1_WAITL(N, b_) asm volatile("s_waitcnt lgkmcnt(" #N ")" : "+v"(xf[b_]), "+v"(wl[b_][0]), "+v"(wh[b_][0]), "+v"(wl[b_][1]), "+v"(wh[b_][1]), "+v"(wl[b_][2]), "+v"(wh[b_][2]) :: "memory")
  WAIT_V(15);
  BARRIER();
  STAMP(0, 1);
  rd(0, 0, 0);
  rd(0, 1, 1);
#pragma unroll
  for (int it = 0; it < 8; ++it) {
#pragma unroll
    for (int s = 0; s < 4; ++s) {
      const int buf = s & 1;
      if (it < 7 || s < 3) K1_WAITL(7, buf); else K1_WAITL(0, buf);
#pragma unroll
      for (int i = 0; i < 3; ++i) {
        const bf16x8 wf = __builtin_shufflevector(wl[buf][i], wh[buf][i], 0, 1, 2, 3, 4, 5, 6, 7);
        acc[i] = MFMA32(wf, xf[buf], acc[i]);
      }
      if (s == 0 && it < 7) {
        if (it == 0) WAIT_V(10);
        else if (it <= 5) WAIT_V(5);
        else __builtin_amdgcn_s_waitcnt(0x0F70);
        BARRIER();
        if (it == 3) STAMP(0, 2);
        if (it == 4) {
#pragma unroll
          for (int i = 0; i < 2; ++i) {
            int f0 = 64 * pidx[1 + i] + 32 * wc;
            f0 = f0 < F_ - 32 ? f0 : F_ - 32;
#pragma unroll
            for (int g = 0; g < 4; ++g) bb[i][g] = *(const u32x2*)(b1 + f0 + 8 * g + 4 * h);
          }
          asm volatile("" ::: "memory");
        }
        if (it >= 1 && it + 3 < 8 && wc == 0) issue(it + 3);
        if (it == 6) {
  {
    const int fc = fid < NFRAG_W ? fid : 0;
    const bool isw2 = fc < NFRAG_W2;
    const int f2 = isw2 ? fc : fc - NFRAG_W2;
    const unsigned short* src = isw2 ? w2 : wo;
    const int s = f2 >> 4, c = f2 & 15;
#pragma unroll
    for (int j = 0; j < 8; ++j) {
      const int k = 16 * s + (isw2 ? ((j & 3) + 8 * (j >> 2) + 4 * h) : (8 * h + j));
      rt_v[j] = src[k * 512 + 32 * c + l31];
    }
  }
  gx = *(const u32x4*)(x + gtok * 512 + lane * 8);
  gw = *(const u32x4*)(gate_w + lane * 8);

          asm volatile("" ::: "memory");
        }
      }
      if (s < 2) rd(it, s + 2, buf);
      else if (it < 7) rd(it + 1, s - 2, buf);
      if (s == 2 && it >= 1 && it + 3 < 8 && wc == 1) issue(it + 3);
    }
  }

  STAMP(0, 3);
  const int tok = row0 + 32 * wr + l31;
#pragma unroll
  for (int i = 0; i < 3; ++i) {
    if (!pkind[i]) {
      const int gst = 2 * pidx[i] + wc;
      const float sc = gst < 16 ? 0.25f * 1.4426950408889634f : 1.0f;
#pragma unroll
      for (int e = 0; e < 2; ++e) {
        float v[8];
#pragma unroll
        for (int j = 0; j < 8; ++j) v[j] = acc[i][8 * e + j] * sc;
        *(bf16x8*)(ws + OFF_QKV + ((size_t)(2 * gst + e) * 2048 + tok) * 32 + h * 16) = pack8(v);
      }
    } else if (i > 0) {
      const int hs = 2 * pidx[i] + wc;
      if (hs < 43) {
#pragma unroll
        for (int e = 0; e < 2; ++e) {
          float v[8];
#pragma unroll
          for (int g2 = 0; g2 < 2; ++g2) {
            const u32x2 bq = bb[i > 0 ? i - 1 : 0][2 * e + g2];
            const float bv[4] = {__uint_as_float(bq[0] << 16), __uint_as_float(bq[0] & 0xffff0000u),
                                 __uint_as_float(bq[1] << 16), __uint_as_float(bq[1] & 0xffff0000u)};
#pragma unroll
            for (int j = 0; j < 4; ++j) {
              const float hv = acc[i][8 * e + 4 * g2 + j] + bv[j];
              const float ex = __builtin_amdgcn_exp2f(fminf(hv, 20.f) * 1.4426950408889634f);
              const float t = ex * (ex + 2.f);
              v[4 * g2 + j] = hv * t * __builtin_amdgcn_rcpf(t + 2.f);
            }
          }
          *(bf16x8*)(ws + OFF_ACT + ((size_t)((rt * 4 + wr) * KS2 + 2 * hs + e) * 64 + lane) * 16) = pack8(v);
        }
      }
    }
  }
  STAMP(0, 4);
  if (fid < NFRAG_W) {
    u32x4 o;
#pragma unroll
    for (int j = 0; j < 4; ++j) o[j] = (unsigned)rt_v[2 * j] | ((unsigned)rt_v[2 * j + 1] << 16);
    *(u32x4*)(ws + OFF_WF + (size_t)fid * 1024 + lane * 16) = o;
  }
  {
    float s = 0.f;
#pragma unroll
    for (int j = 0; j < 4; ++j) {
      s += __uint_as_float(gx[j] << 16) * __uint_as_float(gw[j] << 16);
      s += __uint_as_float(gx[j] & 0xffff0000u) * __uint_as_float(gw[j] & 0xffff0000u);
    }
#pragma unroll
    for (int o = 32; o >= 1; o >>= 1) s += __shfl_xor(s, o);
    if (lane == 0) ((float*)(ws + OFF_G))[gtok] = __builtin_amdgcn_rcpf(1.f + __builtin_amdgcn_exp2f(-s * 1.4426950408889634f));
  }
  if (b == 0 && tid < 32) *(unsigned*)(ws + OFF_CNT + tid * 256) = 0u;
  STAMP(0, 5); CSTAMP(0, 7);
}

constexpr int RED_STRIDE = 272;
constexpr int RED_TILE = 64 * RED_STRIDE;
constexpr int ONES_OFF = 4 * RED_TILE;
constexpr int K2_LDS = ONES_OFF + 10752 + 64;

__global__ void __launch_bounds__(512, 2)
k2_attn_mlp(char* __restrict__ ws, unsigned short* __restrict__ out) {
  __shared__ __attribute__((aligned(1024))) char lds[K2_LDS];
  const int tid = threadIdx.x, lane = tid & 63;
  const int w = __builtin_amdgcn_readfirstlane(tid >> 6);
  const int b = blockIdx.x;
  const int xcd = b & 7, idx = b >> 3;
  const int rt = xcd * 4 + (idx >> 3);
  const int jt = idx & 7;
  const int h = lane >> 5, l31 = lane & 31;
  unsigned* cnt = (unsigned*)(ws + OFF_CNT + rt * 256);
  unsigned* lds_words = (unsigned*)(lds + ONES_OFF + 10752) - 16;
  const __amdgpu_buffer_rsrc_t orsrc = __builtin_amdgcn_make_buffer_rsrc(ws + OFF_O, 0, 64 * 32 * 1024, 0x00020000);
  const float* gate = (const float*)(ws + OFF_G);

  STAMP(1, 0);
  for (int i = tid; i < 672; i += 512) *(u32x4*)(lds + ONES_OFF + i * 16) = u32x4{0x3F803F80u, 0x3F803F80u, 0x3F803F80u, 0x3F803F80u};
  if (tid == 0) lds_words[16] = 0u;
  __syncthreads();

  f32x16 acc[2][2];
  if (w >= 4) {
    __builtin_amdgcn_s_setprio(2);
    const int head = 4 * jt + (w - 4);
    char* vl = lds + (w - 4) * 10240;
    const char* qb = ws + OFF_QKV + (size_t)head * 2048 * 32;
    const char* kb = qb + (size_t)32 * 2048 * 32;
    const char* vb = kb + (size_t)32 * 2048 * 32;
    const int blk0 = 2 * rt - 8;
    bf16x8 kf[10], qf[2];
#pragma unroll
    for (int i = 0; i < 10; ++i) {
      const int gb = blk0 + i < 0 ? 0 : blk0 + i;
      GLDS16(vb + gb * 1024 + lane * 16, vl + i * 1024);
    }
#pragma unroll
    for (int u = 0; u < 2; ++u) qf[u] = *(const bf16x8*)(qb + (2 * rt + u) * 1024 + l31 * 32 + h * 16);
#pragma unroll
    for (int i = 0; i < 10; ++i) {
      const int gb = blk0 + i < 0 ? 0 : blk0 + i;
      kf[i] = *(const bf16x8*)(kb + gb * 1024 + l31 * 32 + h * 16);
    }
    float gq[2];
#pragma unroll
    for (int u = 0; u < 2; ++u) gq[u] = gate[64 * rt + 32 * u + l31];
    __builtin_amdgcn_s_waitcnt(0x0F70);
    STAMP(1, 1);
    const int tq = (lane & 15) >> 2, tp = lane & 3;
    const bool onesl = (lane >> 4) & 1;
    const char* vrd = lds + (onesl ? (ONES_OFF + 8 * tp) : ((w - 4) * 10240 + (4 * h + tq) * 32 + 8 * tp));
    const int z = l31 - 4 * h;
    auto attend = [&](auto allv) {
    constexpr bool ALLV = decltype(allv)::value;
#pragma unroll
    for (int u = 0; u < 2; ++u) {
      const int ifirst = ALLV ? 0 : (-(blk0 + u) > 0 ? -(blk0 + u) : 0);
      float m = -3.0e38f;
      f32x16 zero;
#pragma unroll
      for (int r = 0; r < 16; ++r) zero[r] = 0.f;
      f32x16 sn = MFMA32(kf[u + 8], qf[u], zero);
#pragma unroll
      for (int i = 8; i >= 0; --i) {
        if (ALLV || i >= ifirst) {
          f32x16 s = sn;
          if (i > 0) sn = MFMA32(kf[u + i - 1], qf[u], zero);
          if (i == 0) {
#pragma unroll
            for (int r = 0; r < 16; ++r) s[r] = ((r & 3) + 8 * (r >> 2) > z) ? s[r] : -3.0e38f;
          }
          if (i == 8) {
#pragma unroll
            for (int r = 0; r < 16; ++r) s[r] = ((r & 3) + 8 * (r >> 2) <= z) ? s[r] : -3.0e38f;
          }
#pragma unroll
          for (int r = 0; r < 16; r += 2) m = fmaxf(fmaxf(m, s[r]), s[r + 1]);
          __builtin_amdgcn_sched_barrier(0);
        }
      }
      m = fmaxf(m, __shfl_xor(m, 32));
      f32x16 negm, o;
#pragma unroll
      for (int r = 0; r < 16; ++r) { negm[r] = -m; o[r] = 0.f; }
      sn = MFMA32(kf[u + 8], qf[u], negm);
#pragma unroll
      for (int i = 8; i >= 0; --i) {
        if (ALLV || i >= ifirst) {
          f32x16 s = sn;
          if (i > 0) sn = MFMA32(kf[u + i - 1], qf[u], negm);
          bf16x8 vf[2];
#pragma unroll
          for (int e = 0; e < 2; ++e) {
            const bf16x4 lo = __builtin_amdgcn_ds_read_tr16_b64_v4bf16((LAS bf16x4*)(vrd + (u + i) * 1024 + e * 512));
            const bf16x4 hi = __builtin_amdgcn_ds_read_tr16_b64_v4bf16((LAS bf16x4*)(vrd + (u + i) * 1024 + e * 512 + 256));
            vf[e] = __builtin_shufflevector(lo, hi, 0, 1, 2, 3, 4, 5, 6, 7);
          }
          if (i == 0) {
#pragma unroll
            for (int r = 0; r < 16; ++r) s[r] = ((r & 3) + 8 * (r >> 2) > z) ? s[r] : -3.0e38f;
          }
          if (i == 8) {
#pragma unroll
            for (int r = 0; r < 16; ++r) s[r] = ((r & 3) + 8 * (r >> 2) <= z) ? s[r] : -3.0e38f;
          }
          float p[16];
#pragma unroll
          for (int r = 0; r < 16; ++r) p[r] = __builtin_amdgcn_exp2f(s[r]);
#pragma unroll
          for (int e = 0; e < 2; ++e) o = MFMA32(vf[e], pack8(p + 8 * e), o);
          __builtin_amdgcn_sched_barrier(0);
        }
      }
      const float inv = gq[u] * __builtin_amdgcn_rcpf(o[8]);
      float ov[8];
#pragma unroll
      for (int j = 0; j < 8; ++j) ov[j] = o[j] * inv;
      const bf16x8 ob = pack8(ov);
      __builtin_amdgcn_raw_buffer_store_b128(__builtin_bit_cast(u32x4, ob), orsrc, (((2 * rt + u) * 32 + head) * 64 + lane) * 16, 0, 16);
    }
    };
    if (blk0 >= 0) attend(std::true_type{}); else attend(std::false_type{});
    STAMP(1, 2);
    WAIT_V(0);
    STAMP(1, 3);
    if (lane == 0) {
      const unsigned old = __hip_atomic_fetch_add((LAS unsigned*)(lds_words + 16), 1u, __ATOMIC_RELAXED, __HIP_MEMORY_SCOPE_WORKGROUP);
      if (old == 3u) __hip_atomic_fetch_add(cnt, 1u, __ATOMIC_RELAXED, __HIP_MEMORY_SCOPE_AGENT);
    }
    if (w == 4) {
      if (lane == 0) {
        unsigned spins = 0;
        while (__hip_atomic_load(cnt, __ATOMIC_RELAXED, __HIP_MEMORY_SCOPE_AGENT) < 8u) {
          __builtin_amdgcn_s_sleep(2);
          if (++spins > (1u << 22)) break;
        }
      }
    }
    BARRIER();
    STAMP(1, 4);
  } else {
    __builtin_amdgcn_s_sleep(8);
    const char* af = ws + OFF_ACT + (size_t)(2 * rt) * KS2 * 1024 + lane * 16;
    const char* wf = ws + OFF_WF + (size_t)(2 * jt) * 1024 + lane * 16;
    const int s0 = w < 2 ? 22 * w : 44 + 21 * (w - 2);
    const int ns = w < 2 ? 22 : 21;
    constexpr int RD = 8;
    bf16x8 ra[RD][2], rb[RD][2];
#pragma unroll
    for (int c = 0; c < 2; ++c)
#pragma unroll
      for (int t = 0; t < 2; ++t)
#pragma unroll
        for (int r = 0; r < 16; ++r) acc[c][t][r] = 0.f;
#pragma unroll
    for (int i = 0; i < RD; ++i) {
      const int s = s0 + i;
      ra[i][0] = *(const bf16x8*)(wf + (size_t)s * 16384);
      ra[i][1] = *(const bf16x8*)(wf + (size_t)s * 16384 + 1024);
      rb[i][0] = *(const bf16x8*)(af + s * 1024);
      rb[i][1] = *(const bf16x8*)(af + s * 1024 + KS2 * 1024);
    }
    const char* wof = ws + OFF_WF + (size_t)(NFRAG_W2 + 2 * jt) * 1024 + lane * 16;
#pragma unroll
    for (int i = 0; i < 22; ++i) {
      const int sl = i % RD;
      if (i < 21 || ns == 22) {
#pragma unroll
        for (int c = 0; c < 2; ++c)
#pragma unroll
          for (int t = 0; t < 2; ++t) acc[c][t] = MFMA32(ra[sl][c], rb[sl][t], acc[c][t]);
      }
      if (i + RD < 22) {
        int s = s0 + i + RD;
        s = s < KS2 ? s : KS2 - 1;
        ra[sl][0] = *(const bf16x8*)(wf + (size_t)s * 16384);
        ra[sl][1] = *(const bf16x8*)(wf + (size_t)s * 16384 + 1024);
        rb[sl][0] = *(const bf16x8*)(af + s * 1024);
        rb[sl][1] = *(const bf16x8*)(af + s * 1024 + KS2 * 1024);
      } else {
        const int hh = 8 * w + (i + RD - 22);
        ra[sl][0] = *(const bf16x8*)(wof + (size_t)hh * 16384);
        ra[sl][1] = *(const bf16x8*)(wof + (size_t)hh * 16384 + 1024);
      }
      __builtin_amdgcn_sched_barrier(0);
    }
    STAMP(1, 2);
#pragma unroll
    for (int t = 0; t < 2; ++t) {
      const float og = 1.f - gate[64 * rt + 32 * t + l31];
#pragma unroll
      for (int c = 0; c < 2; ++c)
#pragma unroll
        for (int r = 0; r < 16; ++r) acc[c][t][r] *= og;
    }
    STAMP(1, 3);
    BARRIER();
    STAMP(1, 4);
#pragma unroll
    for (int i = 0; i < 8; ++i) {
      const int sl = (14 + i) % RD;
#pragma unroll
      for (int t = 0; t < 2; ++t)
        rb[sl][t] = __builtin_bit_cast(bf16x8, __builtin_amdgcn_raw_buffer_load_b128(orsrc, (((2 * rt + t) * 32 + 8 * w + i) * 64 + lane) * 16, 0, 16));
    }
#pragma unroll
    for (int i = 0; i < 8; ++i) {
      const int sl = (14 + i) % RD;
#pragma unroll
      for (int c = 0; c < 2; ++c)
#pragma unroll
        for (int t = 0; t < 2; ++t) acc[c][t] = MFMA32(ra[sl][c], rb[sl][t], acc[c][t]);
    }
    STAMP(1, 5);
    char* red = lds + w * RED_TILE;
#pragma unroll
    for (int c = 0; c < 2; ++c)
#pragma unroll
      for (int t = 0; t < 2; ++t)
#pragma unroll
        for (int g = 0; g < 4; ++g) {
          f32x4 v = {acc[c][t][4 * g], acc[c][t][4 * g + 1], acc[c][t][4 * g + 2], acc[c][t][4 * g + 3]};
          *(f32x4*)(red + (32 * t + l31) * RED_STRIDE + (32 * c + 8 * g + 4 * h) * 4) = v;
        }
  }
  __syncthreads();
  {
    const int tok = tid >> 3, c8 = tid & 7;
    float v[8];
#pragma unroll
    for (int j = 0; j < 8; ++j) v[j] = 0.f;
#pragma unroll
    for (int p = 0; p < 4; ++p) {
      const f32x4 a = *(const f32x4*)(lds + p * RED_TILE + tok * RED_STRIDE + c8 * 32);
      const f32x4 c = *(const f32x4*)(lds + p * RED_TILE + tok * RED_STRIDE + c8 * 32 + 16);
#pragma unroll
      for (int j = 0; j < 4; ++j) { v[j] += a[j]; v[4 + j] += c[j]; }
    }
    *(bf16x8*)(out + (size_t)(64 * rt + tok) * 512 + 64 * jt + c8 * 8) = pack8(v);
  }
  STAMP(1, 6);
}

extern "C" void kernel_launch(void* const* d_in, const int* in_sizes, int n_in, void* d_out, int out_size, void* d_ws,
                              size_t ws_size, hipStream_t stream) {
  const unsigned short* x = (const unsigned short*)d_in[0];
  const unsigned short* wqkv = (const unsigned short*)d_in[1];
  const unsigned short* wo = (const unsigned short*)d_in[2];
  const unsigned short* w1 = (const unsigned short*)d_in[4];
  const unsigned short* b1 = (const unsigned short*)d_in[5];
  const unsigned short* w2 = (const unsigned short*)d_in[6];
  const unsigned short* gw = (const unsigned short*)d_in[7];
  k1_proj<<<256, 512, 0, stream>>>(x, wqkv, wo, w1, b1, w2, gw, (char*)d_ws);
  k2_attn_mlp<<<256, 512, 0, stream>>>((char*)d_ws, (unsigned short*)d_out);
}
```

```cpp
#include <hip/hip_runtime.h>
#include <hip/hip_bf16.h>
#include <type_traits>

#define LAS __attribute__((address_space(3)))
#define GAS __attribute__((address_space(1)))
typedef __attribute__((ext_vector_type(8))) __bf16 bf16x8;
typedef __attribute__((ext_vector_type(4))) __bf16 bf16x4;
typedef __attribute__((ext_vector_type(16))) float f32x16;
typedef __attribute__((ext_vector_type(4))) float f32x4;
typedef __attribute__((ext_vector_type(4))) unsigned u32x4;
typedef __attribute__((ext_vector_type(2))) unsigned u32x2;

constexpr int S_ = 2048, D_ = 512, F_ = 1376;
constexpr int KS2 = F_ / 16;
constexpr int NFRAG_W2 = KS2 * 16;
constexpr int NFRAG_W = NFRAG_W2 + 32 * 16;
constexpr size_t OFF_QKV = 0;
constexpr size_t OFF_ACT = OFF_QKV + (size_t)96 * 2048 * 32;
constexpr size_t OFF_WF = OFF_ACT + (size_t)64 * KS2 * 1024;
constexpr size_t OFF_O = OFF_WF + (size_t)NFRAG_W * 1024;
constexpr size_t OFF_G = OFF_O + (size_t)64 * 32 * 1024;
constexpr size_t OFF_CNT = OFF_G + 2048 * 4;
constexpr size_t OFF_F1 = OFF_CNT, OFF_F2 = OFF_CNT + 1024, OFF_EPOCH = OFF_CNT + 2048;
constexpr int WS_BYTES = (int)(OFF_CNT + 4096);

constexpr size_t OFF_STAMP = OFF_CNT + 8192;
#define STAMP(kk, i) do {} while (0)
#define CSTAMP(kk, i) do {} while (0)
#define ROT (rt + ct)
constexpr int RED_STRIDE = 272;
constexpr int RED_TILE = 64 * RED_STRIDE;
constexpr int ONES_OFF = 4 * RED_TILE;
constexpr int K2_LDS = ONES_OFF + 10752 + 64;

#define MFMA32(a, b, c) __builtin_amdgcn_mfma_f32_32x32x16_bf16((a), (b), (c), 0, 0, 0)
#define WAIT_V(n) asm volatile("s_waitcnt vmcnt(" #n ")" ::: "memory")
#define BARRIER() do { __builtin_amdgcn_s_barrier(); asm volatile("" ::: "memory"); } while (0)
#define GLDS16(g, l) __builtin_amdgcn_global_load_lds((const GAS void*)(g), (LAS void*)(l), 16, 0, 0)

#define RLX_AGENT __ATOMIC_RELAXED, __HIP_MEMORY_SCOPE_AGENT
#define ST16(off, v) __builtin_amdgcn_raw_buffer_store_b128(__builtin_bit_cast(u32x4, (v)), wsr, (int)(off), 0, 16)
#define LD16(off) __builtin_bit_cast(bf16x8, __builtin_amdgcn_raw_buffer_load_b128(wsr, (int)(off), 0, 16))
__device__ __forceinline__ float bf2f(unsigned short v) { return __uint_as_float(((unsigned)v) << 16); }
__device__ __forceinline__ bf16x8 pack8(const float* v) {
  bf16x8 r;
#pragma unroll
  for (int j = 0; j < 8; ++j) r[j] = (__bf16)v[j];
  return r;
}

__device__ __forceinline__ void phase2(char* __restrict__ ws, const __amdgpu_buffer_rsrc_t wsr, unsigned short* __restrict__ out, char* lds, unsigned* lds_words, const unsigned tag);
__global__ void __launch_bounds__(512, 2)
fused(const unsigned short* __restrict__ x, const unsigned short* __restrict__ wqkv, const unsigned short* __restrict__ wo,
        const unsigned short* __restrict__ w1, const unsigned short* __restrict__ b1, const unsigned short* __restrict__ w2,
        const unsigned short* __restrict__ gate_w, char* __restrict__ ws, unsigned short* __restrict__ out) {
  __shared__ __attribute__((aligned(1024))) char lds[4 * 40960];
  const int tid = threadIdx.x, lane = tid & 63;
  const int w = __builtin_amdgcn_readfirstlane(tid >> 6);
  const int b = blockIdx.x;
  const int xcd = b & 7, idx = b >> 3;
  const int rt = (xcd & 1) * 8 + (idx & 7);
  const int ct = (xcd >> 1) * 4 + (idx >> 3);
  const int h = lane >> 5, l31 = lane & 31;
  const int row0 = rt * 128;

  STAMP(0, 0); CSTAMP(0, 6);
  const __amdgpu_buffer_rsrc_t wsr = __builtin_amdgcn_make_buffer_rsrc(ws, 0, WS_BYTES, 0x00020000);
  const unsigned tag = __hip_atomic_load((unsigned*)(ws + OFF_EPOCH), RLX_AGENT) + 1u;
  asm volatile("" ::: "memory");
  unsigned short rt_v[8];
  const int fid = b * 8 + w;
  const int gtok = row0 + ct * 8 + w;
  u32x4 gx, gw;
  const int m3 = 3 * (ct >> 1), odd = ct & 1;
  int pidx[3];
  bool pkind[3];
  pidx[0] = odd ? m3 + 2 : m3;      pkind[0] = false;
  pidx[1] = m3 + 1;                 pkind[1] = odd;
  pidx[2] = odd ? m3 + 2 : m3;      pkind[2] = true;
  const unsigned short* xg[2];
#pragma unroll
  for (int p = 0; p < 2; ++p) {
    const int r = (2 * w + p) * 8 + (lane >> 3);
    const int ch = (lane & 7) ^ ((r >> 1) & 7);
    xg[p] = x + (row0 + r) * 512 + ch * 8;
  }
  const unsigned short* wg[3];
  int wstep[3];
#pragma unroll
  for (int cp = 0; cp < 3; ++cp) {
    const int ldw = pkind[cp] ? F_ : 1536;
    const int k = 8 * w + (lane >> 3);
    const int cc = lane & 7;
    const int half = (cc >> 2) ^ ((lane >> 4) & 1);
    int col = 64 * pidx[cp] + 32 * half + 8 * (cc & 3);
    col = col < ldw - 8 ? col : ldw - 8;
    wg[cp] = (pkind[cp] ? w1 : wqkv) + k * ldw + col;
    wstep[cp] = 64 * ldw;
  }
  auto issue = [&](int stage) {
    char* base = lds + (stage & 3) * 40960;
    const int ks = (stage + ROT) & 7;
#pragma unroll
    for (int p = 0; p < 2; ++p) GLDS16(xg[p] + ks * 64, base + (2 * w + p) * 1024);
#pragma unroll
    for (int cp = 0; cp < 3; ++cp) GLDS16(wg[cp] + ks * wstep[cp], base + 16384 + cp * 8192 + w * 1024);
  };
  issue(0); issue(1); issue(2); issue(3);
  asm volatile("" ::: "memory");
  const int wr = w & 3, wc = w >> 2;
  u32x2 bb[2][4];
  f32x16 acc[3];
#pragma unroll
  for (int i = 0; i < 3; ++i)
#pragma unroll
    for (int r = 0; r < 16; ++r) acc[i][r] = 0.f;
  const unsigned ldsbase = (unsigned)(size_t)(LAS char*)lds;
  unsigned xoff[4];
  {
    const int xrow = 32 * wr + l31;
    const int xsw = (xrow >> 1) & 7;
#pragma unroll
    for (int s = 0; s < 4; ++s) xoff[s] = ldsbase + xrow * 128 + (((2 * s + h) ^ xsw) << 4);
  }
  unsigned woff[3];
  {
    const int blk = (lane >> 4) & 1, q = (lane & 15) >> 2, p = lane & 3;
#pragma unroll
    for (int i = 0; i < 3; ++i)
      woff[i] = ldsbase + 16384 + i * 8192 + (8 * h + q) * 128 + ((wc ^ ((q >> 1) & 1)) << 6) + 32 * blk + 8 * p;
  }
  bf16x8 xf[2];
  bf16x4 wl[2][3], wh[2][3];
  auto rd = [&](int it, int s, int buf) {
    const unsigned so = (it & 3) * 40960;
    asm volatile("ds_read_b128 %0, %1" : "=v"(xf[buf]) : "v"(xoff[s] + so) : "memory");
#pragma unroll
    for (int i = 0; i < 3; ++i) {
      asm volatile("ds_read_b64_tr_b16 %0, %1 offset:%2" : "=v"(wl[buf][i]) : "v"(woff[i] + so), "n"(s * 2048) : "memory");
      asm volatile("ds_read_b64_tr_b16 %0, %1 offset:%2" : "=v"(wh[buf][i]) : "v"(woff[i] + so), "n"(s * 2048 + 512) : "memory");
    }
  };
#define K1_WAITL(N, b_) asm volatile("s_waitcnt lgkmcnt(" #N ")" : "+v"(xf[b_]), "+v"(wl[b_][0]), "+v"(wh[b_][0]), "+v"(wl[b_][1]), "+v"(wh[b_][1]), "+v"(wl[b_][2]), "+v"(wh[b_][2]) :: "memory")
  WAIT_V(15);
  BARRIER();
  STAMP(0, 1);
  rd(0, 0, 0);
  rd(0, 1, 1);
#pragma unroll
  for (int it = 0; it < 8; ++it) {
#pragma unroll
    for (int s = 0; s < 4; ++s) {
      const int buf = s & 1;
      if (it < 7 || s < 3) K1_WAITL(7, buf); else K1_WAITL(0, buf);
#pragma unroll
      for (int i = 0; i < 3; ++i) {
        const bf16x8 wf = __builtin_shufflevector(wl[buf][i], wh[buf][i], 0, 1, 2, 3, 4, 5, 6, 7);
        acc[i] = MFMA32(wf, xf[buf], acc[i]);
      }
      if (s == 0 && it < 7) {
        if (it == 0) WAIT_V(10);
        else if (it <= 5) WAIT_V(5);
        else __builtin_amdgcn_s_waitcnt(0x0F70);
        BARRIER();
        if (it == 3) STAMP(0, 2);
        if (it == 4) {
#pragma unroll
          for (int i = 0; i < 2; ++i) {
            int f0 = 64 * pidx[1 + i] + 32 * wc;
            f0 = f0 < F_ - 32 ? f0 : F_ - 32;
#pragma unroll
            for (int g = 0; g < 4; ++g) bb[i][g] = *(const u32x2*)(b1 + f0 + 8 * g + 4 * h);
          }
          asm volatile("" ::: "memory");
        }
        if (it >= 1 && it + 3 < 8 && wc == 0) issue(it + 3);
        if (it == 6) {
  {
    const int fc = fid < NFRAG_W ? fid : 0;
    const bool isw2 = fc < NFRAG_W2;
    const int f2 = isw2 ? fc : fc - NFRAG_W2;
    const unsigned short* src = isw2 ? w2 : wo;
    const int s = f2 >> 4, c = f2 & 15;
#pragma unroll
    for (int j = 0; j < 8; ++j) {
      const int k = 16 * s + (isw2 ? ((j & 3) + 8 * (j >> 2) + 4 * h) : (8 * h + j));
      rt_v[j] = src[k * 512 + 32 * c + l31];
    }
  }
  gx = *(const u32x4*)(x + gtok * 512 + lane * 8);
  gw = *(const u32x4*)(gate_w + lane * 8);

          asm volatile("" ::: "memory");
        }
      }
      if (s < 2) rd(it, s + 2, buf);
      else if (it < 7) rd(it + 1, s - 2, buf);
      if (s == 2 && it >= 1 && it + 3 < 8 && wc == 1) issue(it + 3);
    }
  }

  STAMP(0, 3);
  const int tok = row0 + 32 * wr + l31;
#pragma unroll
  for (int i = 0; i < 3; ++i) {
    if (!pkind[i]) {
      const int gst = 2 * pidx[i] + wc;
      const float sc = gst < 16 ? 0.25f * 1.4426950408889634f : 1.0f;
#pragma unroll
      for (int e = 0; e < 2; ++e) {
        float v[8];
#pragma unroll
        for (int j = 0; j < 8; ++j) v[j] = acc[i][8 * e + j] * sc;
        ST16(OFF_QKV + ((2 * gst + e) * 2048 + tok) * 32 + h * 16, pack8(v));
      }
    } else if (i > 0) {
      const int hs = 2 * pidx[i] + wc;
      if (hs < 43) {
#pragma unroll
        for (int e = 0; e < 2; ++e) {
          float v[8];
#pragma unroll
          for (int g2 = 0; g2 < 2; ++g2) {
            const u32x2 bq = bb[i > 0 ? i - 1 : 0][2 * e + g2];
            const float bv[4] = {__uint_as_float(bq[0] << 16), __uint_as_float(bq[0] & 0xffff0000u),
                                 __uint_as_float(bq[1] << 16), __uint_as_float(bq[1] & 0xffff0000u)};
#pragma unroll
            for (int j = 0; j < 4; ++j) {
              const float hv = acc[i][8 * e + 4 * g2 + j] + bv[j];
              const float ex = __builtin_amdgcn_exp2f(fminf(hv, 20.f) * 1.4426950408889634f);
              const float t = ex * (ex + 2.f);
              v[4 * g2 + j] = hv * t * __builtin_amdgcn_rcpf(t + 2.f);
            }
          }
          ST16(OFF_ACT + (((rt * 4 + wr) * KS2 + 2 * hs + e) * 64 + lane) * 16, pack8(v));
        }
      }
    }
  }
  STAMP(0, 4);
  if (fid < NFRAG_W) {
    u32x4 o;
#pragma unroll
    for (int j = 0; j < 4; ++j) o[j] = (unsigned)rt_v[2 * j] | ((unsigned)rt_v[2 * j + 1] << 16);
    ST16(OFF_WF + fid * 1024 + lane * 16, o);
  }
  {
    float s = 0.f;
#pragma unroll
    for (int j = 0; j < 4; ++j) {
      s += __uint_as_float(gx[j] << 16) * __uint_as_float(gw[j] << 16);
      s += __uint_as_float(gx[j] & 0xffff0000u) * __uint_as_float(gw[j] & 0xffff0000u);
    }
#pragma unroll
    for (int o = 32; o >= 1; o >>= 1) s += __shfl_xor(s, o);
    if (lane == 0) __hip_atomic_store((float*)(ws + OFF_G) + gtok, __builtin_amdgcn_rcpf(1.f + __builtin_amdgcn_exp2f(-s * 1.4426950408889634f)), RLX_AGENT);
  }
  STAMP(0, 5); CSTAMP(0, 7);
  WAIT_V(0);
  BARRIER();
  if (tid == 0) __hip_atomic_store((unsigned*)(ws + OFF_F1) + b, tag, RLX_AGENT);
  for (int i = tid; i < 672; i += 512) *(u32x4*)(lds + ONES_OFF + i * 16) = u32x4{0x3F803F80u, 0x3F803F80u, 0x3F803F80u, 0x3F803F80u};
  unsigned* lds_words = (unsigned*)(lds + ONES_OFF + 10752) - 16;
  if (tid == 0) lds_words[16] = 0u;
  if (w == 0) {
    unsigned spins = 0;
    for (;;) {
      const u32x4 f = __builtin_amdgcn_raw_buffer_load_b128(wsr, (int)OFF_F1 + lane * 16, 0, 16);
      if (__all(f[0] == tag && f[1] == tag && f[2] == tag && f[3] == tag)) break;
      __builtin_amdgcn_s_sleep(1);
      if (++spins > (1u << 20)) break;
    }
    if (b == 0 && lane == 0) __hip_atomic_store((unsigned*)(ws + OFF_EPOCH), tag, RLX_AGENT);
  }
  __syncthreads();
  STAMP(1, 0);
  phase2(ws, wsr, out, lds, lds_words, tag);
}

__device__ __forceinline__ void phase2(char* __restrict__ ws, const __amdgpu_buffer_rsrc_t wsr, unsigned short* __restrict__ out, char* lds, unsigned* lds_words, const unsigned tag) {
  const int tid = threadIdx.x, lane = tid & 63;
  const int w = __builtin_amdgcn_readfirstlane(tid >> 6);
  const int b = blockIdx.x;
  const int xcd = b & 7, idx = b >> 3;
  const int rt = xcd * 4 + (idx >> 3);
  const int jt = idx & 7;
  const int h = lane >> 5, l31 = lane & 31;
  f32x16 acc[2][2];
  if (w >= 4) {
    __builtin_amdgcn_s_setprio(2);
    const int head = 4 * jt + (w - 4);
    char* vl = lds + (w - 4) * 10240;
    const int qb = (int)OFF_QKV + head * 2048 * 32;
    const int kb = qb + 32 * 2048 * 32;
    const int vb = kb + 32 * 2048 * 32;
    const int blk0 = 2 * rt - 8;
    bf16x8 kf[10], qf[2];
#pragma unroll
    for (int u = 0; u < 2; ++u) qf[u] = LD16(qb + (2 * rt + u) * 1024 + l31 * 32 + h * 16);
    bf16x8 vtmp[10];
#pragma unroll
    for (int i = 0; i < 10; ++i) {
      const int gb = blk0 + i < 0 ? 0 : blk0 + i;
      kf[i] = LD16(kb + gb * 1024 + l31 * 32 + h * 16);
      vtmp[i] = LD16(vb + gb * 1024 + lane * 16);
    }
    float gq[2];
#pragma unroll
    for (int u = 0; u < 2; ++u) gq[u] = __hip_atomic_load((const float*)(ws + OFF_G) + 64 * rt + 32 * u + l31, RLX_AGENT);
#pragma unroll
    for (int i = 0; i < 10; ++i) *(bf16x8*)(vl + i * 1024 + lane * 16) = vtmp[i];
    STAMP(1, 1);
    const int tq = (lane & 15) >> 2, tp = lane & 3;
    const bool onesl = (lane >> 4) & 1;
    const char* vrd = lds + (onesl ? (ONES_OFF + 8 * tp) : ((w - 4) * 10240 + (4 * h + tq) * 32 + 8 * tp));
    const int z = l31 - 4 * h;
    auto attend = [&](auto allv) {
    constexpr bool ALLV = decltype(allv)::value;
#pragma unroll
    for (int u = 0; u < 2; ++u) {
      const int ifirst = ALLV ? 0 : (-(blk0 + u) > 0 ? -(blk0 + u) : 0);
      float m = -3.0e38f;
      f32x16 zero;
#pragma unroll
      for (int r = 0; r < 16; ++r) zero[r] = 0.f;
      f32x16 sn = MFMA32(kf[u + 8], qf[u], zero);
#pragma unroll
      for (int i = 8; i >= 0; --i) {
        if (ALLV || i >= ifirst) {
          f32x16 s = sn;
          if (i > 0) sn = MFMA32(kf[u + i - 1], qf[u], zero);
          if (i == 0) {
#pragma unroll
            for (int r = 0; r < 16; ++r) s[r] = ((r & 3) + 8 * (r >> 2) > z) ? s[r] : -3.0e38f;
          }
          if (i == 8) {
#pragma unroll
            for (int r = 0; r < 16; ++r) s[r] = ((r & 3) + 8 * (r >> 2) <= z) ? s[r] : -3.0e38f;
          }
#pragma unroll
          for (int r = 0; r < 16; r += 2) m = fmaxf(fmaxf(m, s[r]), s[r + 1]);
          __builtin_amdgcn_sched_barrier(0);
        }
      }
      m = fmaxf(m, __shfl_xor(m, 32));
      f32x16 negm, o;
#pragma unroll
      for (int r = 0; r < 16; ++r) { negm[r] = -m; o[r] = 0.f; }
      sn = MFMA32(kf[u + 8], qf[u], negm);
#pragma unroll
      for (int i = 8; i >= 0; --i) {
        if (ALLV || i >= ifirst) {
          f32x16 s = sn;
          if (i > 0) sn = MFMA32(kf[u + i - 1], qf[u], negm);
          bf16x8 vf[2];
#pragma unroll
          for (int e = 0; e < 2; ++e) {
            const bf16x4 lo = __builtin_amdgcn_ds_read_tr16_b64_v4bf16((LAS bf16x4*)(vrd + (u + i) * 1024 + e * 512));
            const bf16x4 hi = __builtin_amdgcn_ds_read_tr16_b64_v4bf16((LAS bf16x4*)(vrd + (u + i) * 1024 + e * 512 + 256));
            vf[e] = __builtin_shufflevector(lo, hi, 0, 1, 2, 3, 4, 5, 6, 7);
          }
          if (i == 0) {
#pragma unroll
            for (int r = 0; r < 16; ++r) s[r] = ((r & 3) + 8 * (r >> 2) > z) ? s[r] : -3.0e38f;
          }
          if (i == 8) {
#pragma unroll
            for (int r = 0; r < 16; ++r) s[r] = ((r & 3) + 8 * (r >> 2) <= z) ? s[r] : -3.0e38f;
          }
          float p[16];
#pragma unroll
          for (int r = 0; r < 16; ++r) p[r] = __builtin_amdgcn_exp2f(s[r]);
#pragma unroll
          for (int e = 0; e < 2; ++e) o = MFMA32(vf[e], pack8(p + 8 * e), o);
          __builtin_amdgcn_sched_barrier(0);
        }
      }
      const float inv = gq[u] * __builtin_amdgcn_rcpf(o[8]);
      float ov[8];
#pragma unroll
      for (int j = 0; j < 8; ++j) ov[j] = o[j] * inv;
      const bf16x8 ob = pack8(ov);
      ST16(OFF_O + (((2 * rt + u) * 32 + head) * 64 + lane) * 16, ob);
    }
    };
    if (blk0 >= 0) attend(std::true_type{}); else attend(std::false_type{});
    STAMP(1, 2);
    WAIT_V(0);
    STAMP(1, 3);
    if (lane == 0) {
      const unsigned old = __hip_atomic_fetch_add((LAS unsigned*)(lds_words + 16), 1u, __ATOMIC_RELAXED, __HIP_MEMORY_SCOPE_WORKGROUP);
      if (old == 3u) __hip_atomic_store((unsigned*)(ws + OFF_F2) + rt * 8 + jt, tag, RLX_AGENT);
    }
    if (w == 4) {
      unsigned spins = 0;
      for (;;) {
        const unsigned f = __hip_atomic_load((const unsigned*)(ws + OFF_F2) + rt * 8 + (lane & 7), RLX_AGENT);
        if (__all(f == tag)) break;
        __builtin_amdgcn_s_sleep(1);
        if (++spins > (1u << 20)) break;
      }
    }
    BARRIER();
    STAMP(1, 4);
  } else {
    __builtin_amdgcn_s_sleep(4);
    const int af = (int)OFF_ACT + (2 * rt) * KS2 * 1024 + lane * 16;
    const int wf = (int)OFF_WF + (2 * jt) * 1024 + lane * 16;
    float gv[2];
#pragma unroll
    for (int t = 0; t < 2; ++t) gv[t] = __hip_atomic_load((const float*)(ws + OFF_G) + 64 * rt + 32 * t + l31, RLX_AGENT);
    const int s0 = w < 2 ? 22 * w : 44 + 21 * (w - 2);
    const int ns = w < 2 ? 22 : 21;
    constexpr int RD = 8;
    bf16x8 ra[RD][2], rb[RD][2];
#pragma unroll
    for (int c = 0; c < 2; ++c)
#pragma unroll
      for (int t = 0; t < 2; ++t)
#pragma unroll
        for (int r = 0; r < 16; ++r) acc[c][t][r] = 0.f;
#pragma unroll
    for (int i = 0; i < RD; ++i) {
      const int s = s0 + i;
      ra[i][0] = LD16(wf + s * 16384);
      ra[i][1] = LD16(wf + s * 16384 + 1024);
      rb[i][0] = LD16(af + s * 1024);
      rb[i][1] = LD16(af + s * 1024 + KS2 * 1024);
    }
    const int wof = (int)OFF_WF + (NFRAG_W2 + 2 * jt) * 1024 + lane * 16;
#pragma unroll
    for (int i = 0; i < 22; ++i) {
      const int sl = i % RD;
      if (i < 21 || ns == 22) {
#pragma unroll
        for (int c = 0; c < 2; ++c)
#pragma unroll
          for (int t = 0; t < 2; ++t) acc[c][t] = MFMA32(ra[sl][c], rb[sl][t], acc[c][t]);
      }
      if (i + RD < 22) {
        int s = s0 + i + RD;
        s = s < KS2 ? s : KS2 - 1;
        ra[sl][0] = LD16(wf + s * 16384);
        ra[sl][1] = LD16(wf + s * 16384 + 1024);
        rb[sl][0] = LD16(af + s * 1024);
        rb[sl][1] = LD16(af + s * 1024 + KS2 * 1024);
      } else {
        const int hh = 8 * w + (i + RD - 22);
        ra[sl][0] = LD16(wof + hh * 16384);
        ra[sl][1] = LD16(wof + hh * 16384 + 1024);
      }
      __builtin_amdgcn_sched_barrier(0);
    }
    STAMP(1, 2);
#pragma unroll
    for (int t = 0; t < 2; ++t) {
      const float og = 1.f - gv[t];
#pragma unroll
      for (int c = 0; c < 2; ++c)
#pragma unroll
        for (int r = 0; r < 16; ++r) acc[c][t][r] *= og;
    }
    STAMP(1, 3);
    BARRIER();
    STAMP(1, 4);
#pragma unroll
    for (int i = 0; i < 8; ++i) {
      const int sl = (14 + i) % RD;
#pragma unroll
      for (int t = 0; t < 2; ++t)
        rb[sl][t] = LD16(OFF_O + (((2 * rt + t) * 32 + 8 * w + i) * 64 + lane) * 16);
    }
#pragma unroll
    for (int i = 0; i < 8; ++i) {
      const int sl = (14 + i) % RD;
#pragma unroll
      for (int c = 0; c < 2; ++c)
#pragma unroll
        for (int t = 0; t < 2; ++t) acc[c][t] = MFMA32(ra[sl][c], rb[sl][t], acc[c][t]);
    }
    STAMP(1, 5);
    char* red = lds + w * RED_TILE;
#pragma unroll
    for (int c = 0; c < 2; ++c)
#pragma unroll
      for (int t = 0; t < 2; ++t)
#pragma unroll
        for (int g = 0; g < 4; ++g) {
          f32x4 v = {acc[c][t][4 * g], acc[c][t][4 * g + 1], acc[c][t][4 * g + 2], acc[c][t][4 * g + 3]};
          *(f32x4*)(red + (32 * t + l31) * RED_STRIDE + (32 * c + 8 * g + 4 * h) * 4) = v;
        }
  }
  __syncthreads();
  {
    const int tok = tid >> 3, c8 = tid & 7;
    float v[8];
#pragma unroll
    for (int j = 0; j < 8; ++j) v[j] = 0.f;
#pragma unroll
    for (int p = 0; p < 4; ++p) {
      const f32x4 a = *(const f32x4*)(lds + p * RED_TILE + tok * RED_STRIDE + c8 * 32);
      const f32x4 c = *(const f32x4*)(lds + p * RED_TILE + tok * RED_STRIDE + c8 * 32 + 16);
#pragma unroll
      for (int j = 0; j < 4; ++j) { v[j] += a[j]; v[4 + j] += c[j]; }
    }
    *(bf16x8*)(out + (size_t)(64 * rt + tok) * 512 + 64 * jt + c8 * 8) = pack8(v);
  }
  STAMP(1, 6);
}

extern "C" void kernel_launch(void* const* d_in, const int* in_sizes, int n_in, void* d_out, int out_size, void* d_ws,
                              size_t ws_size, hipStream_t stream) {
  const unsigned short* x = (const unsigned short*)d_in[0];
  const unsigned short* wqkv = (const unsigned short*)d_in[1];
  const unsigned short* wo = (const unsigned short*)d_in[2];
  const unsigned short* w1 = (const unsigned short*)d_in[4];
  const unsigned short* b1 = (const unsigned short*)d_in[5];
  const unsigned short* w2 = (const unsigned short*)d_in[6];
  const unsigned short* gw = (const unsigned short*)d_in[7];
  fused<<<256, 512, 0, stream>>>(x, wqkv, wo, w1, b1, w2, gw, (char*)d_ws, (unsigned short*)d_out);
}
```

```cpp
#include <hip/hip_runtime.h>
#include <hip/hip_bf16.h>
#include <type_traits>

#define LAS __attribute__((address_space(3)))
#define GAS __attribute__((address_space(1)))
typedef __attribute__((ext_vector_type(8))) __bf16 bf16x8;
typedef __attribute__((ext_vector_type(4))) __bf16 bf16x4;
typedef __attribute__((ext_vector_type(16))) float f32x16;
typedef __attribute__((ext_vector_type(4))) float f32x4;
typedef __attribute__((ext_vector_type(4))) unsigned u32x4;
typedef __attribute__((ext_vector_type(2))) unsigned u32x2;

constexpr int S_ = 2048, D_ = 512, F_ = 1376;
constexpr int KS2 = F_ / 16;
constexpr int NFRAG_W2 = KS2 * 16;
constexpr int NFRAG_W = NFRAG_W2 + 32 * 16;
constexpr size_t OFF_QKV = 0;
constexpr size_t OFF_ACT = OFF_QKV + (size_t)96 * 2048 * 32;
constexpr size_t OFF_WF = OFF_ACT + (size_t)64 * KS2 * 1024;
constexpr size_t OFF_O = OFF_WF + (size_t)NFRAG_W * 1024;
constexpr size_t OFF_G = OFF_O + (size_t)64 * 32 * 1024;
constexpr size_t OFF_CNT = OFF_G + 2048 * 4;

constexpr size_t OFF_STAMP = OFF_CNT + 32 * 256;
#define STAMP(kk, i) do {} while (0)
#define CSTAMP(kk, i) do {} while (0)
#define ROT (rt + ct)
#define SLP 8
#define GPRIO 0
#define STAUX 16
#define ST16(off, v) __builtin_amdgcn_raw_buffer_store_b128(__builtin_bit_cast(u32x4, (v)), wsr, (int)(off), 0, STAUX)
#define MFMA32(a, b, c) __builtin_amdgcn_mfma_f32_32x32x16_bf16((a), (b), (c), 0, 0, 0)
#define WAIT_V(n) asm volatile("s_waitcnt vmcnt(" #n ")" ::: "memory")
#define BARRIER() do { __builtin_amdgcn_s_barrier(); asm volatile("" ::: "memory"); } while (0)
#define GLDS16(g, l) __builtin_amdgcn_global_load_lds((const GAS void*)(g), (LAS void*)(l), 16, 0, 0)

__device__ __forceinline__ float bf2f(unsigned short v) { return __uint_as_float(((unsigned)v) << 16); }
__device__ __forceinline__ bf16x8 pack8(const float* v) {
  bf16x8 r;
#pragma unroll
  for (int j = 0; j < 8; ++j) r[j] = (__bf16)v[j];
  return r;
}

__global__ void __launch_bounds__(512, 2)
k1_proj(const unsigned short* __restrict__ x, const unsigned short* __restrict__ wqkv, const unsigned short* __restrict__ wo,
        const unsigned short* __restrict__ w1, const unsigned short* __restrict__ b1, const unsigned short* __restrict__ w2,
        const unsigned short* __restrict__ gate_w, char* __restrict__ ws) {
  __shared__ __attribute__((aligned(1024))) char lds[4 * 40960];
  const int tid = threadIdx.x, lane = tid & 63;
  const int w = __builtin_amdgcn_readfirstlane(tid >> 6);
  const int b = blockIdx.x;
  const int xcd = b & 7, idx = b >> 3;
  const int rt = (xcd & 1) * 8 + (idx & 7);
  const int ct = (xcd >> 1) * 4 + (idx >> 3);
  const int h = lane >> 5, l31 = lane & 31;
  const int row0 = rt * 128;

  STAMP(0, 0); CSTAMP(0, 6);
  const __amdgpu_buffer_rsrc_t wsr = __builtin_amdgcn_make_buffer_rsrc(ws, 0, (int)OFF_G, 0x00020000);
  asm volatile("" ::: "memory");
  unsigned short rt_v[8];
  const int fid = b * 8 + w;
  const int gtok = row0 + ct * 8 + w;
  u32x4 gx, gw;
  const int m3 = 3 * (ct >> 1), odd = ct & 1;
  int pidx[3];
  bool pkind[3];
  pidx[0] = odd ? m3 + 2 : m3;      pkind[0] = false;
  pidx[1] = m3 + 1;                 pkind[1] = odd;
  pidx[2] = odd ? m3 + 2 : m3;      pkind[2] = true;
  const unsigned short* xg[2];
#pragma unroll
  for (int p = 0; p < 2; ++p) {
    const int r = (2 * w + p) * 8 + (lane >> 3);
    const int ch = (lane & 7) ^ ((r >> 1) & 7);
    xg[p] = x + (row0 + r) * 512 + ch * 8;
  }
  const unsigned short* wg[3];
  int wstep[3];
#pragma unroll
  for (int cp = 0; cp < 3; ++cp) {
    const int ldw = pkind[cp] ? F_ : 1536;
    const int k = 8 * w + (lane >> 3);
    const int cc = lane & 7;
    const int half = (cc >> 2) ^ ((lane >> 4) & 1);
    int col = 64 * pidx[cp] + 32 * half + 8 * (cc & 3);
    col = col < ldw - 8 ? col : ldw - 8;
    wg[cp] = (pkind[cp] ? w1 : wqkv) + k * ldw + col;
    wstep[cp] = 64 * ldw;
  }
  auto issue = [&](int stage) {
    char* base = lds + (stage & 3) * 40960;
    const int ks = (stage + ROT) & 7;
#pragma unroll
    for (int p = 0; p < 2; ++p) GLDS16(xg[p] + ks * 64, base + (2 * w + p) * 1024);
#pragma unroll
    for (int cp = 0; cp < 3; ++cp) GLDS16(wg[cp] + ks * wstep[cp], base + 16384 + cp * 8192 + w * 1024);
  };
  issue(0);
  BARRIER();
  issue(1); issue(2); issue(3);
  asm volatile("" ::: "memory");
  const int wr = w & 3, wc = w >> 2;
  u32x2 bb[2][4];
  f32x16 acc[3];
#pragma unroll
  for (int i = 0; i < 3; ++i)
#pragma unroll
    for (int r = 0; r < 16; ++r) acc[i][r] = 0.f;
  const unsigned ldsbase = (unsigned)(size_t)(LAS char*)lds;
  unsigned xoff[4];
  {
    const int xrow = 32 * wr + l31;
    const int xsw = (xrow >> 1) & 7;
#pragma unroll
    for (int s = 0; s < 4; ++s) xoff[s] = ldsbase + xrow * 128 + (((2 * s + h) ^ xsw) << 4);
  }
  unsigned woff[3];
  {
    const int blk = (lane >> 4) & 1, q = (lane & 15) >> 2, p = lane & 3;
#pragma unroll
    for (int i = 0; i < 3; ++i)
      woff[i] = ldsbase + 16384 + i * 8192 + (8 * h + q) * 128 + ((wc ^ ((q >> 1) & 1)) << 6) + 32 * blk + 8 * p;
  }
  bf16x8 xf[2];
  bf16x4 wl[2][3], wh[2][3];
  auto rd = [&](int it, int s, int buf) {
    const unsigned so = (it & 3) * 40960;
    asm volatile("ds_read_b128 %0, %1" : "=v"(xf[buf]) : "v"(xoff[s] + so) : "memory");
#pragma unroll
    for (int i = 0; i < 3; ++i) {
      asm volatile("ds_read_b64_tr_b16 %0, %1 offset:%2" : "=v"(wl[buf][i]) : "v"(woff[i] + so), "n"(s * 2048) : "memory");
      asm volatile("ds_read_b64_tr_b16 %0, %1 offset:%2" : "=v"(wh[buf][i]) : "v"(woff[i] + so), "n"(s * 2048 + 512) : "memory");
    }
  };
#define K1_WAITL(N, b_) asm volatile("s_waitcnt lgkmcnt(" #N ")" : "+v"(xf[b_]), "+v"(wl[b_][0]), "+v"(wh[b_][0]), "+v"(wl[b_][1]), "+v"(wh[b_][1]), "+v"(wl[b_][2]), "+v"(wh[b_][2]) :: "memory")
  WAIT_V(15);
  BARRIER();
  STAMP(0, 1);
  rd(0, 0, 0);
  rd(0, 1, 1);
#pragma unroll
  for (int it = 0; it < 8; ++it) {
#pragma unroll
    for (int s = 0; s < 4; ++s) {
      const int buf = s & 1;
      if (it < 7 || s < 3) K1_WAITL(7, buf); else K1_WAITL(0, buf);
#pragma unroll
      for (int i = 0; i < 3; ++i) {
        const bf16x8 wf = __builtin_shufflevector(wl[buf][i], wh[buf][i], 0, 1, 2, 3, 4, 5, 6, 7);
        acc[i] = MFMA32(wf, xf[buf], acc[i]);
      }
      if (s == 0 && it < 7) {
        if (it == 0) WAIT_V(10);
        else if (it <= 5) WAIT_V(5);
        else __builtin_amdgcn_s_waitcnt(0x0F70);
        BARRIER();
        if (it == 3) STAMP(0, 2);
        if (it == 4) {
#pragma unroll
          for (int i = 0; i < 2; ++i) {
            int f0 = 64 * pidx[1 + i] + 32 * wc;
            f0 = f0 < F_ - 32 ? f0 : F_ - 32;
#pragma unroll
            for (int g = 0; g < 4; ++g) bb[i][g] = *(const u32x2*)(b1 + f0 + 8 * g + 4 * h);
          }
          asm volatile("" ::: "memory");
        }
        if (it >= 1 && it + 3 < 8 && wc == 0) issue(it + 3);
        if (it == 6) {
  {
    const int fc = fid < NFRAG_W ? fid : 0;
    const bool isw2 = fc < NFRAG_W2;
    const int f2 = isw2 ? fc : fc - NFRAG_W2;
    const unsigned short* src = isw2 ? w2 : wo;
    const int s = f2 >> 4, c = f2 & 15;
#pragma unroll
    for (int j = 0; j < 8; ++j) {
      const int k = 16 * s + (isw2 ? ((j & 3) + 8 * (j >> 2) + 4 * h) : (8 * h + j));
      rt_v[j] = src[k * 512 + 32 * c + l31];
    }
  }
  gx = *(const u32x4*)(x + gtok * 512 + lane * 8);
  gw = *(const u32x4*)(gate_w + lane * 8);

          asm volatile("" ::: "memory");
        }
      }
      if (s < 2) rd(it, s + 2, buf);
      else if (it < 7) rd(it + 1, s - 2, buf);
      if (s == 2 && it >= 1 && it + 3 < 8 && wc == 1) issue(it + 3);
    }
  }

  STAMP(0, 3);
  const int tok = row0 + 32 * wr + l31;
#pragma unroll
  for (int i = 0; i < 3; ++i) {
    if (!pkind[i]) {
      const int gst = 2 * pidx[i] + wc;
      const float sc = gst < 16 ? 0.25f * 1.4426950408889634f : 1.0f;
#pragma unroll
      for (int e = 0; e < 2; ++e) {
        float v[8];
#pragma unroll
        for (int j = 0; j < 8; ++j) v[j] = acc[i][8 * e + j] * sc;
        ST16(OFF_QKV + ((2 * gst + e) * 2048 + tok) * 32 + h * 16, pack8(v));
      }
    } else if (i > 0) {
      const int hs = 2 * pidx[i] + wc;
      if (hs < 43) {
#pragma unroll
        for (int e = 0; e < 2; ++e) {
          float v[8];
#pragma unroll
          for (int g2 = 0; g2 < 2; ++g2) {
            const u32x2 bq = bb[i > 0 ? i - 1 : 0][2 * e + g2];
            const float bv[4] = {__uint_as_float(bq[0] << 16), __uint_as_float(bq[0] & 0xffff0000u),
                                 __uint_as_float(bq[1] << 16), __uint_as_float(bq[1] & 0xffff0000u)};
#pragma unroll
            for (int j = 0; j < 4; ++j) {
              const float hv = acc[i][8 * e + 4 * g2 + j] + bv[j];
              const float ex = __builtin_amdgcn_exp2f(fminf(hv, 20.f) * 1.4426950408889634f);
              const float t = ex * (ex + 2.f);
              v[4 * g2 + j] = hv * t * __builtin_amdgcn_rcpf(t + 2.f);
            }
          }
          ST16(OFF_ACT + (((rt * 4 + wr) * KS2 + 2 * hs + e) * 64 + lane) * 16, pack8(v));
        }
      }
    }
  }
  STAMP(0, 4);
  if (fid < NFRAG_W) {
    u32x4 o;
#pragma unroll
    for (int j = 0; j < 4; ++j) o[j] = (unsigned)rt_v[2 * j] | ((unsigned)rt_v[2 * j + 1] << 16);
    ST16(OFF_WF + fid * 1024 + lane * 16, o);
  }
  {
    float s = 0.f;
#pragma unroll
    for (int j = 0; j < 4; ++j) {
      s += __uint_as_float(gx[j] << 16) * __uint_as_float(gw[j] << 16);
      s += __uint_as_float(gx[j] & 0xffff0000u) * __uint_as_float(gw[j] & 0xffff0000u);
    }
#pragma unroll
    for (int o = 32; o >= 1; o >>= 1) s += __shfl_xor(s, o);
    if (lane == 0) ((float*)(ws + OFF_G))[gtok] = __builtin_amdgcn_rcpf(1.f + __builtin_amdgcn_exp2f(-s * 1.4426950408889634f));
  }
  if (b == 0 && tid < 32) *(unsigned*)(ws + OFF_CNT + tid * 256) = 0u;
  STAMP(0, 5); CSTAMP(0, 7);
}

constexpr int RED_STRIDE = 272;
constexpr int RED_TILE = 64 * RED_STRIDE;
constexpr int ONES_OFF = 4 * RED_TILE;
constexpr int K2_LDS = ONES_OFF + 10752 + 64;

__global__ void __launch_bounds__(512, 2)
k2_attn_mlp(char* __restrict__ ws, unsigned short* __restrict__ out) {
  __shared__ __attribute__((aligned(1024))) char lds[K2_LDS];
  const int tid = threadIdx.x, lane = tid & 63;
  const int w = __builtin_amdgcn_readfirstlane(tid >> 6);
  const int b = blockIdx.x;
  const int xcd = b & 7, idx = b >> 3;
  const int rt = xcd * 4 + (idx >> 3);
  const int jt = idx & 7;
  const int h = lane >> 5, l31 = lane & 31;
  unsigned* cnt = (unsigned*)(ws + OFF_CNT + rt * 256);
  unsigned* lds_words = (unsigned*)(lds + ONES_OFF + 10752) - 16;
  const __amdgpu_buffer_rsrc_t orsrc = __builtin_amdgcn_make_buffer_rsrc(ws + OFF_O, 0, 64 * 32 * 1024, 0x00020000);
  const float* gate = (const float*)(ws + OFF_G);

  STAMP(1, 0);
  for (int i = tid; i < 672; i += 512) *(u32x4*)(lds + ONES_OFF + i * 16) = u32x4{0x3F803F80u, 0x3F803F80u, 0x3F803F80u, 0x3F803F80u};
  if (tid == 0) lds_words[16] = 0u;
  __syncthreads();

  f32x16 acc[2][2];
  if (w >= 4) {
    __builtin_amdgcn_s_setprio(2);
    const int head = 4 * jt + (w - 4);
    char* vl = lds + (w - 4) * 10240;
    const char* qb = ws + OFF_QKV + (size_t)head * 2048 * 32;
    const char* kb = qb + (size_t)32 * 2048 * 32;
    const char* vb = kb + (size_t)32 * 2048 * 32;
    const int blk0 = 2 * rt - 8;
    bf16x8 kf[10], qf[2];
#pragma unroll
    for (int i = 0; i < 10; ++i) {
      const int gb = blk0 + i < 0 ? 0 : blk0 + i;
      GLDS16(vb + gb * 1024 + lane * 16, vl + i * 1024);
    }
#pragma unroll
    for (int u = 0; u < 2; ++u) qf[u] = *(const bf16x8*)(qb + (2 * rt + u) * 1024 + l31 * 32 + h * 16);
#pragma unroll
    for (int i = 0; i < 10; ++i) {
      const int gb = blk0 + i < 0 ? 0 : blk0 + i;
      kf[i] = *(const bf16x8*)(kb + gb * 1024 + l31 * 32 + h * 16);
    }
    float gq[2];
#pragma unroll
    for (int u = 0; u < 2; ++u) gq[u] = gate[64 * rt + 32 * u + l31];
    __builtin_amdgcn_s_waitcnt(0x0F70);
    STAMP(1, 1);
    const int tq = (lane & 15) >> 2, tp = lane & 3;
    const bool onesl = (lane >> 4) & 1;
    const char* vrd = lds + (onesl ? (ONES_OFF + 8 * tp) : ((w - 4) * 10240 + (4 * h + tq) * 32 + 8 * tp));
    const int z = l31 - 4 * h;
    auto attend = [&](auto allv) {
      constexpr bool ALLV = decltype(allv)::value;
      const int jfirst = ALLV ? 0 : -blk0;
      f32x16 zero;
#pragma unroll
      for (int r = 0; r < 16; ++r) zero[r] = 0.f;
      float m[2][2] = {{-3.0e38f, -3.0e38f}, {-3.0e38f, -3.0e38f}};
#pragma unroll
      for (int j = 9; j >= 0; --j) {
        if (ALLV || j >= jfirst) {
#pragma unroll
          for (int u = 0; u < 2; ++u) {
            const int i = j - u;
            if (i >= 0 && i <= 8) {
              f32x16 s = MFMA32(kf[j], qf[u], zero);
              if (i == 0) {
#pragma unroll
                for (int r = 0; r < 16; ++r) s[r] = ((r & 3) + 8 * (r >> 2) > z) ? s[r] : -3.0e38f;
              }
              if (i == 8) {
#pragma unroll
                for (int r = 0; r < 16; ++r) s[r] = ((r & 3) + 8 * (r >> 2) <= z) ? s[r] : -3.0e38f;
              }
#pragma unroll
              for (int r = 0; r < 16; r += 4) {
                m[u][0] = fmaxf(fmaxf(m[u][0], s[r]), s[r + 1]);
                m[u][1] = fmaxf(fmaxf(m[u][1], s[r + 2]), s[r + 3]);
              }
            }
          }
          if (j & 1) __builtin_amdgcn_sched_barrier(0);
        }
      }
      f32x16 negm[2], o[2];
#pragma unroll
      for (int u = 0; u < 2; ++u) {
        float mm = fmaxf(m[u][0], m[u][1]);
        mm = fmaxf(mm, __shfl_xor(mm, 32));
#pragma unroll
        for (int r = 0; r < 16; ++r) { negm[u][r] = -mm; o[u][r] = 0.f; }
      }
#pragma unroll
      for (int j = 9; j >= 0; --j) {
        if (ALLV || j >= jfirst) {
          bf16x8 vf[2];
#pragma unroll
          for (int e = 0; e < 2; ++e) {
            const bf16x4 lo = __builtin_amdgcn_ds_read_tr16_b64_v4bf16((LAS bf16x4*)(vrd + j * 1024 + e * 512));
            const bf16x4 hi = __builtin_amdgcn_ds_read_tr16_b64_v4bf16((LAS bf16x4*)(vrd + j * 1024 + e * 512 + 256));
            vf[e] = __builtin_shufflevector(lo, hi, 0, 1, 2, 3, 4, 5, 6, 7);
          }
          f32x16 s[2];
#pragma unroll
          for (int u = 0; u < 2; ++u)
            if (j - u >= 0 && j - u <= 8) s[u] = MFMA32(kf[j], qf[u], negm[u]);
#pragma unroll
          for (int u = 0; u < 2; ++u) {
            const int i = j - u;
            if (i >= 0 && i <= 8) {
              if (i == 0) {
#pragma unroll
                for (int r = 0; r < 16; ++r) s[u][r] = ((r & 3) + 8 * (r >> 2) > z) ? s[u][r] : -3.0e38f;
              }
              if (i == 8) {
#pragma unroll
                for (int r = 0; r < 16; ++r) s[u][r] = ((r & 3) + 8 * (r >> 2) <= z) ? s[u][r] : -3.0e38f;
              }
              float p[16];
#pragma unroll
              for (int r = 0; r < 16; ++r) p[r] = __builtin_amdgcn_exp2f(s[u][r]);
#pragma unroll
              for (int e = 0; e < 2; ++e) o[u] = MFMA32(vf[e], pack8(p + 8 * e), o[u]);
            }
          }
          __builtin_amdgcn_sched_barrier(0);
        }
      }
#pragma unroll
      for (int u = 0; u < 2; ++u) {
        const float inv = gq[u] * __builtin_amdgcn_rcpf(o[u][8]);
        float ov[8];
#pragma unroll
        for (int j = 0; j < 8; ++j) ov[j] = o[u][j] * inv;
        const bf16x8 ob = pack8(ov);
        __builtin_amdgcn_raw_buffer_store_b128(__builtin_bit_cast(u32x4, ob), orsrc, (((2 * rt + u) * 32 + head) * 64 + lane) * 16, 0, 16);
      }
    };
    if (blk0 >= 0) attend(std::true_type{}); else attend(std::false_type{});
    STAMP(1, 2);
    WAIT_V(0);
    STAMP(1, 3);
    if (lane == 0) {
      const unsigned old = __hip_atomic_fetch_add((LAS unsigned*)(lds_words + 16), 1u, __ATOMIC_RELAXED, __HIP_MEMORY_SCOPE_WORKGROUP);
      if (old == 3u) __hip_atomic_fetch_add(cnt, 1u, __ATOMIC_RELAXED, __HIP_MEMORY_SCOPE_AGENT);
    }
    if (w == 4) {
      if (lane == 0) {
        unsigned spins = 0;
        while (__hip_atomic_load(cnt, __ATOMIC_RELAXED, __HIP_MEMORY_SCOPE_AGENT) < 8u) {
          __builtin_amdgcn_s_sleep(2);
          if (++spins > (1u << 22)) break;
        }
      }
    }
    BARRIER();
    STAMP(1, 4);
  } else {
    __builtin_amdgcn_s_sleep(SLP);
    __builtin_amdgcn_s_setprio(GPRIO);
    const char* af = ws + OFF_ACT + (size_t)(2 * rt) * KS2 * 1024 + lane * 16;
    const char* wf = ws + OFF_WF + (size_t)(2 * jt) * 1024 + lane * 16;
    const int s0 = w < 2 ? 22 * w : 44 + 21 * (w - 2);
    const int ns = w < 2 ? 22 : 21;
    constexpr int RD = 8;
    bf16x8 ra[RD][2], rb[RD][2];
#pragma unroll
    for (int c = 0; c < 2; ++c)
#pragma unroll
      for (int t = 0; t < 2; ++t)
#pragma unroll
        for (int r = 0; r < 16; ++r) acc[c][t][r] = 0.f;
#pragma unroll
    for (int i = 0; i < RD; ++i) {
      const int s = s0 + i;
      ra[i][0] = *(const bf16x8*)(wf + (size_t)s * 16384);
      ra[i][1] = *(const bf16x8*)(wf + (size_t)s * 16384 + 1024);
      rb[i][0] = *(const bf16x8*)(af + s * 1024);
      rb[i][1] = *(const bf16x8*)(af + s * 1024 + KS2 * 1024);
    }
    const char* wof = ws + OFF_WF + (size_t)(NFRAG_W2 + 2 * jt) * 1024 + lane * 16;
#pragma unroll
    for (int i = 0; i < 22; ++i) {
      const int sl = i % RD;
      if (i < 21 || ns == 22) {
#pragma unroll
        for (int c = 0; c < 2; ++c)
#pragma unroll
          for (int t = 0; t < 2; ++t) acc[c][t] = MFMA32(ra[sl][c], rb[sl][t], acc[c][t]);
      }
      if (i + RD < 22) {
        int s = s0 + i + RD;
        s = s < KS2 ? s : KS2 - 1;
        ra[sl][0] = *(const bf16x8*)(wf + (size_t)s * 16384);
        ra[sl][1] = *(const bf16x8*)(wf + (size_t)s * 16384 + 1024);
        rb[sl][0] = *(const bf16x8*)(af + s * 1024);
        rb[sl][1] = *(const bf16x8*)(af + s * 1024 + KS2 * 1024);
      } else {
        const int hh = 8 * w + (i + RD - 22);
        ra[sl][0] = *(const bf16x8*)(wof + (size_t)hh * 16384);
        ra[sl][1] = *(const bf16x8*)(wof + (size_t)hh * 16384 + 1024);
      }
      __builtin_amdgcn_sched_barrier(0);
    }
    STAMP(1, 2);
#pragma unroll
    for (int t = 0; t < 2; ++t) {
      const float og = 1.f - gate[64 * rt + 32 * t + l31];
#pragma unroll
      for (int c = 0; c < 2; ++c)
#pragma unroll
        for (int r = 0; r < 16; ++r) acc[c][t][r] *= og;
    }
    STAMP(1, 3);
    BARRIER();
    STAMP(1, 4);
#pragma unroll
    for (int i = 0; i < 8; ++i) {
      const int sl = (14 + i) % RD;
#pragma unroll
      for (int t = 0; t < 2; ++t)
        rb[sl][t] = __builtin_bit_cast(bf16x8, __builtin_amdgcn_raw_buffer_load_b128(orsrc, (((2 * rt + t) * 32 + 8 * w + i) * 64 + lane) * 16, 0, 16));
    }
#pragma unroll
    for (int i = 0; i < 8; ++i) {
      const int sl = (14 + i) % RD;
#pragma unroll
      for (int c = 0; c < 2; ++c)
#pragma unroll
        for (int t = 0; t < 2; ++t) acc[c][t] = MFMA32(ra[sl][c], rb[sl][t], acc[c][t]);
    }
    STAMP(1, 5);
    char* red = lds + w * RED_TILE;
#pragma unroll
    for (int c = 0; c < 2; ++c)
#pragma unroll
      for (int t = 0; t < 2; ++t)
#pragma unroll
        for (int g = 0; g < 4; ++g) {
          f32x4 v = {acc[c][t][4 * g], acc[c][t][4 * g + 1], acc[c][t][4 * g + 2], acc[c][t][4 * g + 3]};
          *(f32x4*)(red + (32 * t + l31) * RED_STRIDE + (32 * c + 8 * g + 4 * h) * 4) = v;
        }
  }
  __syncthreads();
  {
    const int tok = tid >> 3, c8 = tid & 7;
    float v[8];
#pragma unroll
    for (int j = 0; j < 8; ++j) v[j] = 0.f;
#pragma unroll
    for (int p = 0; p < 4; ++p) {
      const f32x4 a = *(const f32x4*)(lds + p * RED_TILE + tok * RED_STRIDE + c8 * 32);
      const f32x4 c = *(const f32x4*)(lds + p * RED_TILE + tok * RED_STRIDE + c8 * 32 + 16);
#pragma unroll
      for (int j = 0; j < 4; ++j) { v[j] += a[j]; v[4 + j] += c[j]; }
    }
    *(bf16x8*)(out + (size_t)(64 * rt + tok) * 512 + 64 * jt + c8 * 8) = pack8(v);
  }
  STAMP(1, 6);
}

extern "C" void kernel_launch(void* const* d_in, const int* in_sizes, int n_in, void* d_out, int out_size, void* d_ws,
                              size_t ws_size, hipStream_t stream) {
  const unsigned short* x = (const unsigned short*)d_in[0];
  const unsigned short* wqkv = (const unsigned short*)d_in[1];
  const unsigned short* wo = (const unsigned short*)d_in[2];
  const unsigned short* w1 = (const unsigned short*)d_in[4];
  const unsigned short* b1 = (const unsigned short*)d_in[5];
  const unsigned short* w2 = (const unsigned short*)d_in[6];
  const unsigned short* gw = (const unsigned short*)d_in[7];
  k1_proj<<<256, 512, 0, stream>>>(x, wqkv, wo, w1, b1, w2, gw, (char*)d_ws);
  k2_attn_mlp<<<256, 512, 0, stream>>>((char*)d_ws, (unsigned short*)d_out);
}
```

```cpp
#include <hip/hip_runtime.h>
#include <hip/hip_bf16.h>
#include <type_traits>

#define LAS __attribute__((address_space(3)))
#define GAS __attribute__((address_space(1)))
typedef __attribute__((ext_vector_type(8))) __bf16 bf16x8;
typedef __attribute__((ext_vector_type(4))) __bf16 bf16x4;
typedef __attribute__((ext_vector_type(16))) float f32x16;
typedef __attribute__((ext_vector_type(4))) float f32x4;
typedef __attribute__((ext_vector_type(4))) unsigned u32x4;
typedef __attribute__((ext_vector_type(2))) unsigned u32x2;

constexpr int S_ = 2048, D_ = 512, F_ = 1376;
constexpr int KS2 = F_ / 16;
constexpr int NFRAG_W2 = KS2 * 16;
constexpr int NFRAG_W = NFRAG_W2 + 32 * 16;
constexpr size_t OFF_QKV = 0;
constexpr size_t OFF_ACT = OFF_QKV + (size_t)96 * 2048 * 32;
constexpr size_t OFF_WF = OFF_ACT + (size_t)64 * KS2 * 1024;
constexpr size_t OFF_O = OFF_WF + (size_t)NFRAG_W * 1024;
constexpr size_t OFF_G = OFF_O + (size_t)64 * 32 * 1024;
constexpr size_t OFF_CNT = OFF_G + 2048 * 4;

#define ROT (rt + ct)
#define SLP 8
#define GPRIO 0
#define STAUX 16
#define ST16(off, v) __builtin_amdgcn_raw_buffer_store_b128(__builtin_bit_cast(u32x4, (v)), wsr, (int)(off), 0, STAUX)
#define MFMA32(a, b, c) __builtin_amdgcn_mfma_f32_32x32x16_bf16((a), (b), (c), 0, 0, 0)
#define WAIT_V(n) asm volatile("s_waitcnt vmcnt(" #n ")" ::: "memory")
#define BARRIER() do { __builtin_amdgcn_s_barrier(); asm volatile("" ::: "memory"); } while (0)
#define GLDS16(g, l) __builtin_amdgcn_global_load_lds((const GAS void*)(g), (LAS void*)(l), 16, 0, 0)

__device__ __forceinline__ float bf2f(unsigned short v) { return __uint_as_float(((unsigned)v) << 16); }
__device__ __forceinline__ bf16x8 pack8(const float* v) {
  bf16x8 r;
#pragma unroll
  for (int j = 0; j < 8; ++j) r[j] = (__bf16)v[j];
  return r;
}

__global__ void __launch_bounds__(512, 2)
k1_proj(const unsigned short* __restrict__ x, const unsigned short* __restrict__ wqkv, const unsigned short* __restrict__ wo,
        const unsigned short* __restrict__ w1, const unsigned short* __restrict__ b1, const unsigned short* __restrict__ w2,
        const unsigned short* __restrict__ gate_w, char* __restrict__ ws) {
  __shared__ __attribute__((aligned(1024))) char lds[4 * 40960];
  const int tid = threadIdx.x, lane = tid & 63;
  const int w = __builtin_amdgcn_readfirstlane(tid >> 6);
  const int b = blockIdx.x;
  const int xcd = b & 7, idx = b >> 3;
  const int rt = (xcd & 1) * 8 + (idx & 7);
  const int ct = (xcd >> 1) * 4 + (idx >> 3);
  const int h = lane >> 5, l31 = lane & 31;
  const int row0 = rt * 128;
  const __amdgpu_buffer_rsrc_t wsr = __builtin_amdgcn_make_buffer_rsrc(ws, 0, (int)OFF_G, 0x00020000);
  asm volatile("" ::: "memory");
  unsigned short rt_v[8];
  const int fid = b * 8 + w;
  const int gtok = row0 + ct * 8 + w;
  u32x4 gx, gw;
  const int m3 = 3 * (ct >> 1), odd = ct & 1;
  int pidx[3];
  bool pkind[3];
  pidx[0] = odd ? m3 + 2 : m3;      pkind[0] = false;
  pidx[1] = m3 + 1;                 pkind[1] = odd;
  pidx[2] = odd ? m3 + 2 : m3;      pkind[2] = true;
  const unsigned short* xg[2];
#pragma unroll
  for (int p = 0; p < 2; ++p) {
    const int r = (2 * w + p) * 8 + (lane >> 3);
    const int ch = (lane & 7) ^ ((r >> 1) & 7);
    xg[p] = x + (row0 + r) * 512 + ch * 8;
  }
  const unsigned short* wg[3];
  int wstep[3];
#pragma unroll
  for (int cp = 0; cp < 3; ++cp) {
    const int ldw = pkind[cp] ? F_ : 1536;
    const int k = 8 * w + (lane >> 3);
    const int cc = lane & 7;
    const int half = (cc >> 2) ^ ((lane >> 4) & 1);
    int col = 64 * pidx[cp] + 32 * half + 8 * (cc & 3);
    col = col < ldw - 8 ? col : ldw - 8;
    wg[cp] = (pkind[cp] ? w1 : wqkv) + k * ldw + col;
    wstep[cp] = 64 * ldw;
  }
  auto issue = [&](int stage) {
    char* base = lds + (stage & 3) * 40960;
    const int ks = (stage + ROT) & 7;
#pragma unroll
    for (int p = 0; p < 2; ++p) GLDS16(xg[p] + ks * 64, base + (2 * w + p) * 1024);
#pragma unroll
    for (int cp = 0; cp < 3; ++cp) GLDS16(wg[cp] + ks * wstep[cp], base + 16384 + cp * 8192 + w * 1024);
  };
  issue(0);
  BARRIER();
  issue(1); issue(2); issue(3);
  asm volatile("" ::: "memory");
  const int wr = w & 3, wc = w >> 2;
  u32x2 bb[2][4];
  f32x16 acc[3];
#pragma unroll
  for (int i = 0; i < 3; ++i)
#pragma unroll
    for (int r = 0; r < 16; ++r) acc[i][r] = 0.f;
  const unsigned ldsbase = (unsigned)(size_t)(LAS char*)lds;
  unsigned xoff[4];
  {
    const int xrow = 32 * wr + l31;
    const int xsw = (xrow >> 1) & 7;
#pragma unroll
    for (int s = 0; s < 4; ++s) xoff[s] = ldsbase + xrow * 128 + (((2 * s + h) ^ xsw) << 4);
  }
  unsigned woff[3];
  {
    const int blk = (lane >> 4) & 1, q = (lane & 15) >> 2, p = lane & 3;
#pragma unroll
    for (int i = 0; i < 3; ++i)
      woff[i] = ldsbase + 16384 + i * 8192 + (8 * h + q) * 128 + ((wc ^ ((q >> 1) & 1)) << 6) + 32 * blk + 8 * p;
  }
  bf16x8 xf[2];
  bf16x4 wl[2][3], wh[2][3];
  auto rd = [&](int it, int s, int buf) {
    const unsigned so = (it & 3) * 40960;
    asm volatile("ds_read_b128 %0, %1" : "=v"(xf[buf]) : "v"(xoff[s] + so) : "memory");
#pragma unroll
    for (int i = 0; i < 3; ++i) {
      asm volatile("ds_read_b64_tr_b16 %0, %1 offset:%2" : "=v"(wl[buf][i]) : "v"(woff[i] + so), "n"(s * 2048) : "memory");
      asm volatile("ds_read_b64_tr_b16 %0, %1 offset:%2" : "=v"(wh[buf][i]) : "v"(woff[i] + so), "n"(s * 2048 + 512) : "memory");
    }
  };
#define K1_WAITL(N, b_) asm volatile("s_waitcnt lgkmcnt(" #N ")" : "+v"(xf[b_]), "+v"(wl[b_][0]), "+v"(wh[b_][0]), "+v"(wl[b_][1]), "+v"(wh[b_][1]), "+v"(wl[b_][2]), "+v"(wh[b_][2]) :: "memory")
  WAIT_V(15);
  BARRIER();
  rd(0, 0, 0);
  rd(0, 1, 1);
#pragma unroll
  for (int it = 0; it < 8; ++it) {
#pragma unroll
    for (int s = 0; s < 4; ++s) {
      const int buf = s & 1;
      if (it < 7 || s < 3) K1_WAITL(7, buf); else K1_WAITL(0, buf);
#pragma unroll
      for (int i = 0; i < 3; ++i) {
        const bf16x8 wf = __builtin_shufflevector(wl[buf][i], wh[buf][i], 0, 1, 2, 3, 4, 5, 6, 7);
        acc[i] = MFMA32(wf, xf[buf], acc[i]);
      }
      if (s == 0 && it < 7) {
        if (it == 0) WAIT_V(10);
        else if (it <= 5) WAIT_V(5);
        else __builtin_amdgcn_s_waitcnt(0x0F70);
        BARRIER();
        if (it == 4) {
#pragma unroll
          for (int i = 0; i < 2; ++i) {
            int f0 = 64 * pidx[1 + i] + 32 * wc;
            f0 = f0 < F_ - 32 ? f0 : F_ - 32;
#pragma unroll
            for (int g = 0; g < 4; ++g) bb[i][g] = *(const u32x2*)(b1 + f0 + 8 * g + 4 * h);
          }
          asm volatile("" ::: "memory");
        }
        if (it >= 1 && it + 3 < 8 && wc == 0) issue(it + 3);
        if (it == 6) {
  {
    const int fc = fid < NFRAG_W ? fid : 0;
    const bool isw2 = fc < NFRAG_W2;
    const int f2 = isw2 ? fc : fc - NFRAG_W2;
    const unsigned short* src = isw2 ? w2 : wo;
    const int s = f2 >> 4, c = f2 & 15;
#pragma unroll
    for (int j = 0; j < 8; ++j) {
      const int k = 16 * s + (isw2 ? ((j & 3) + 8 * (j >> 2) + 4 * h) : (8 * h + j));
      rt_v[j] = src[k * 512 + 32 * c + l31];
    }
  }
  gx = *(const u32x4*)(x + gtok * 512 + lane * 8);
  gw = *(const u32x4*)(gate_w + lane * 8);

          asm volatile("" ::: "memory");
        }
      }
      if (s < 2) rd(it, s + 2, buf);
      else if (it < 7) rd(it + 1, s - 2, buf);
      if (s == 2 && it >= 1 && it + 3 < 8 && wc == 1) issue(it + 3);
    }
  }
  const int tok = row0 + 32 * wr + l31;
#pragma unroll
  for (int i = 0; i < 3; ++i) {
    if (!pkind[i]) {
      const int gst = 2 * pidx[i] + wc;
      const float sc = gst < 16 ? 0.25f * 1.4426950408889634f : 1.0f;
#pragma unroll
      for (int e = 0; e < 2; ++e) {
        float v[8];
#pragma unroll
        for (int j = 0; j < 8; ++j) v[j] = acc[i][8 * e + j] * sc;
        ST16(OFF_QKV + ((2 * gst + e) * 2048 + tok) * 32 + h * 16, pack8(v));
      }
    } else if (i > 0) {
      const int hs = 2 * pidx[i] + wc;
      if (hs < 43) {
#pragma unroll
        for (int e = 0; e < 2; ++e) {
          float v[8];
#pragma unroll
          for (int g2 = 0; g2 < 2; ++g2) {
            const u32x2 bq = bb[i > 0 ? i - 1 : 0][2 * e + g2];
            const float bv[4] = {__uint_as_float(bq[0] << 16), __uint_as_float(bq[0] & 0xffff0000u),
                                 __uint_as_float(bq[1] << 16), __uint_as_float(bq[1] & 0xffff0000u)};
#pragma unroll
            for (int j = 0; j < 4; ++j) {
              const float hv = acc[i][8 * e + 4 * g2 + j] + bv[j];
              const float ex = __builtin_amdgcn_exp2f(fminf(hv, 20.f) * 1.4426950408889634f);
              const float t = ex * (ex + 2.f);
              v[4 * g2 + j] = hv * t * __builtin_amdgcn_rcpf(t + 2.f);
            }
          }
          ST16(OFF_ACT + (((rt * 4 + wr) * KS2 + 2 * hs + e) * 64 + lane) * 16, pack8(v));
        }
      }
    }
  }
  if (fid < NFRAG_W) {
    u32x4 o;
#pragma unroll
    for (int j = 0; j < 4; ++j) o[j] = (unsigned)rt_v[2 * j] | ((unsigned)rt_v[2 * j + 1] << 16);
    ST16(OFF_WF + fid * 1024 + lane * 16, o);
  }
  {
    float s = 0.f;
#pragma unroll
    for (int j = 0; j < 4; ++j) {
      s += __uint_as_float(gx[j] << 16) * __uint_as_float(gw[j] << 16);
      s += __uint_as_float(gx[j] & 0xffff0000u) * __uint_as_float(gw[j] & 0xffff0000u);
    }
#pragma unroll
    for (int o = 32; o >= 1; o >>= 1) s += __shfl_xor(s, o);
    if (lane == 0) ((float*)(ws + OFF_G))[gtok] = __builtin_amdgcn_rcpf(1.f + __builtin_amdgcn_exp2f(-s * 1.4426950408889634f));
  }
  if (b == 0 && tid < 32) *(unsigned*)(ws + OFF_CNT + tid * 256) = 0u;
}

constexpr int RED_STRIDE = 272;
constexpr int RED_TILE = 64 * RED_STRIDE;
constexpr int ONES_OFF = 4 * RED_TILE;
constexpr int K2_LDS = ONES_OFF + 10752 + 64;

__global__ void __launch_bounds__(512, 2)
k2_attn_mlp(char* __restrict__ ws, unsigned short* __restrict__ out) {
  __shared__ __attribute__((aligned(1024))) char lds[K2_LDS];
  const int tid = threadIdx.x, lane = tid & 63;
  const int w = __builtin_amdgcn_readfirstlane(tid >> 6);
  const int b = blockIdx.x;
  const int xcd = b & 7, idx = b >> 3;
  const int rt = xcd * 4 + (idx >> 3);
  const int jt = idx & 7;
  const int h = lane >> 5, l31 = lane & 31;
  unsigned* cnt = (unsigned*)(ws + OFF_CNT + rt * 256);
  unsigned* lds_words = (unsigned*)(lds + ONES_OFF + 10752) - 16;
  const __amdgpu_buffer_rsrc_t orsrc = __builtin_amdgcn_make_buffer_rsrc(ws + OFF_O, 0, 64 * 32 * 1024, 0x00020000);
  const float* gate = (const float*)(ws + OFF_G);
  for (int i = tid; i < 672; i += 512) *(u32x4*)(lds + ONES_OFF + i * 16) = u32x4{0x3F803F80u, 0x3F803F80u, 0x3F803F80u, 0x3F803F80u};
  if (tid == 0) lds_words[16] = 0u;
  __syncthreads();

  f32x16 acc[2][2];
  if (w >= 4) {
    __builtin_amdgcn_s_setprio(2);
    const int head = 4 * jt + (w - 4);
    char* vl = lds + (w - 4) * 10240;
    const char* qb = ws + OFF_QKV + (size_t)head * 2048 * 32;
    const char* kb = qb + (size_t)32 * 2048 * 32;
    const char* vb = kb + (size_t)32 * 2048 * 32;
    const int blk0 = 2 * rt - 8;
    bf16x8 kf[10], qf[2];
#pragma unroll
    for (int i = 0; i < 10; ++i) {
      const int gb = blk0 + i < 0 ? 0 : blk0 + i;
      GLDS16(vb + gb * 1024 + lane * 16, vl + i * 1024);
    }
#pragma unroll
    for (int u = 0; u < 2; ++u) qf[u] = *(const bf16x8*)(qb + (2 * rt + u) * 1024 + l31 * 32 + h * 16);
#pragma unroll
    for (int i = 0; i < 10; ++i) {
      const int gb = blk0 + i < 0 ? 0 : blk0 + i;
      kf[i] = *(const bf16x8*)(kb + gb * 1024 + l31 * 32 + h * 16);
    }
    float gq[2];
#pragma unroll
    for (int u = 0; u < 2; ++u) gq[u] = gate[64 * rt + 32 * u + l31];
    __builtin_amdgcn_s_waitcnt(0x0F70);
    const int tq = (lane & 15) >> 2, tp = lane & 3;
    const bool onesl = (lane >> 4) & 1;
    const char* vrd = lds + (onesl ? (ONES_OFF + 8 * tp) : ((w - 4) * 10240 + (4 * h + tq) * 32 + 8 * tp));
    const int z = l31 - 4 * h;
    auto attend = [&](auto allv) {
      constexpr bool ALLV = decltype(allv)::value;
      const int jfirst = ALLV ? 0 : -blk0;
      f32x16 zero;
#pragma unroll
      for (int r = 0; r < 16; ++r) zero[r] = 0.f;
      float m[2][2] = {{-3.0e38f, -3.0e38f}, {-3.0e38f, -3.0e38f}};
#pragma unroll
      for (int j = 9; j >= 0; --j) {
        if (ALLV || j >= jfirst) {
#pragma unroll
          for (int u = 0; u < 2; ++u) {
            const int i = j - u;
            if (i >= 0 && i <= 8) {
              f32x16 s = MFMA32(kf[j], qf[u], zero);
              if (i == 0) {
#pragma unroll
                for (int r = 0; r < 16; ++r) s[r] = ((r & 3) + 8 * (r >> 2) > z) ? s[r] : -3.0e38f;
              }
              if (i == 8) {
#pragma unroll
                for (int r = 0; r < 16; ++r) s[r] = ((r & 3) + 8 * (r >> 2) <= z) ? s[r] : -3.0e38f;
              }
#pragma unroll
              for (int r = 0; r < 16; r += 4) {
                m[u][0] = fmaxf(fmaxf(m[u][0], s[r]), s[r + 1]);
                m[u][1] = fmaxf(fmaxf(m[u][1], s[r + 2]), s[r + 3]);
              }
            }
          }
          if (j & 1) __builtin_amdgcn_sched_barrier(0);
        }
      }
      f32x16 negm[2], o[2];
#pragma unroll
      for (int u = 0; u < 2; ++u) {
        float mm = fmaxf(m[u][0], m[u][1]);
        mm = fmaxf(mm, __shfl_xor(mm, 32));
#pragma unroll
        for (int r = 0; r < 16; ++r) { negm[u][r] = -mm; o[u][r] = 0.f; }
      }
#pragma unroll
      for (int j = 9; j >= 0; --j) {
        if (ALLV || j >= jfirst) {
          bf16x8 vf[2];
#pragma unroll
          for (int e = 0; e < 2; ++e) {
            const bf16x4 lo = __builtin_amdgcn_ds_read_tr16_b64_v4bf16((LAS bf16x4*)(vrd + j * 1024 + e * 512));
            const bf16x4 hi = __builtin_amdgcn_ds_read_tr16_b64_v4bf16((LAS bf16x4*)(vrd + j * 1024 + e * 512 + 256));
            vf[e] = __builtin_shufflevector(lo, hi, 0, 1, 2, 3, 4, 5, 6, 7);
          }
          f32x16 s[2];
#pragma unroll
          for (int u = 0; u < 2; ++u)
            if (j - u >= 0 && j - u <= 8) s[u] = MFMA32(kf[j], qf[u], negm[u]);
#pragma unroll
          for (int u = 0; u < 2; ++u) {
            const int i = j - u;
            if (i >= 0 && i <= 8) {
              if (i == 0) {
#pragma unroll
                for (int r = 0; r < 16; ++r) s[u][r] = ((r & 3) + 8 * (r >> 2) > z) ? s[u][r] : -3.0e38f;
              }
              if (i == 8) {
#pragma unroll
                for (int r = 0; r < 16; ++r) s[u][r] = ((r & 3) + 8 * (r >> 2) <= z) ? s[u][r] : -3.0e38f;
              }
              float p[16];
#pragma unroll
              for (int r = 0; r < 16; ++r) p[r] = __builtin_amdgcn_exp2f(s[u][r]);
#pragma unroll
              for (int e = 0; e < 2; ++e) o[u] = MFMA32(vf[e], pack8(p + 8 * e), o[u]);
            }
          }
          __builtin_amdgcn_sched_barrier(0);
        }
      }
#pragma unroll
      for (int u = 0; u < 2; ++u) {
        const float inv = gq[u] * __builtin_amdgcn_rcpf(o[u][8]);
        float ov[8];
#pragma unroll
        for (int j = 0; j < 8; ++j) ov[j] = o[u][j] * inv;
        const bf16x8 ob = pack8(ov);
        __builtin_amdgcn_raw_buffer_store_b128(__builtin_bit_cast(u32x4, ob), orsrc, (((2 * rt + u) * 32 + head) * 64 + lane) * 16, 0, 16);
      }
    };
    if (blk0 >= 0) attend(std::true_type{}); else attend(std::false_type{});
    WAIT_V(0);
    if (lane == 0) {
      const unsigned old = __hip_atomic_fetch_add((LAS unsigned*)(lds_words + 16), 1u, __ATOMIC_RELAXED, __HIP_MEMORY_SCOPE_WORKGROUP);
      if (old == 3u) __hip_atomic_fetch_add(cnt, 1u, __ATOMIC_RELAXED, __HIP_MEMORY_SCOPE_AGENT);
    }
    if (w == 4) {
      if (lane == 0) {
        unsigned spins = 0;
        while (__hip_atomic_load(cnt, __ATOMIC_RELAXED, __HIP_MEMORY_SCOPE_AGENT) < 8u) {
          __builtin_amdgcn_s_sleep(2);
          if (++spins > (1u << 22)) break;
        }
      }
    }
    BARRIER();
  } else {
    __builtin_amdgcn_s_sleep(SLP);
    __builtin_amdgcn_s_setprio(GPRIO);
    const char* af = ws + OFF_ACT + (size_t)(2 * rt) * KS2 * 1024 + lane * 16;
    const char* wf = ws + OFF_WF + (size_t)(2 * jt) * 1024 + lane * 16;
    const int s0 = w < 2 ? 22 * w : 44 + 21 * (w - 2);
    const int ns = w < 2 ? 22 : 21;
    constexpr int RD = 8;
    bf16x8 ra[RD][2], rb[RD][2];
#pragma unroll
    for (int c = 0; c < 2; ++c)
#pragma unroll
      for (int t = 0; t < 2; ++t)
#pragma unroll
        for (int r = 0; r < 16; ++r) acc[c][t][r] = 0.f;
#pragma unroll
    for (int i = 0; i < RD; ++i) {
      const int s = s0 + i;
      ra[i][0] = *(const bf16x8*)(wf + (size_t)s * 16384);
      ra[i][1] = *(const bf16x8*)(wf + (size_t)s * 16384 + 1024);
      rb[i][0] = *(const bf16x8*)(af + s * 1024);
      rb[i][1] = *(const bf16x8*)(af + s * 1024 + KS2 * 1024);
    }
    const char* wof = ws + OFF_WF + (size_t)(NFRAG_W2 + 2 * jt) * 1024 + lane * 16;
#pragma unroll
    for (int i = 0; i < 22; ++i) {
      const int sl = i % RD;
      if (i < 21 || ns == 22) {
#pragma unroll
        for (int c = 0; c < 2; ++c)
#pragma unroll
          for (int t = 0; t < 2; ++t) acc[c][t] = MFMA32(ra[sl][c], rb[sl][t], acc[c][t]);
      }
      if (i + RD < 22) {
        int s = s0 + i + RD;
        s = s < KS2 ? s : KS2 - 1;
        ra[sl][0] = *(const bf16x8*)(wf + (size_t)s * 16384);
        ra[sl][1] = *(const bf16x8*)(wf + (size_t)s * 16384 + 1024);
        rb[sl][0] = *(const bf16x8*)(af + s * 1024);
        rb[sl][1] = *(const bf16x8*)(af + s * 1024 + KS2 * 1024);
      } else {
        const int hh = 8 * w + (i + RD - 22);
        ra[sl][0] = *(const bf16x8*)(wof + (size_t)hh * 16384);
        ra[sl][1] = *(const bf16x8*)(wof + (size_t)hh * 16384 + 1024);
      }
      __builtin_amdgcn_sched_barrier(0);
    }
#pragma unroll
    for (int t = 0; t < 2; ++t) {
      const float og = 1.f - gate[64 * rt + 32 * t + l31];
#pragma unroll
      for (int c = 0; c < 2; ++c)
#pragma unroll
        for (int r = 0; r < 16; ++r) acc[c][t][r] *= og;
    }
    BARRIER();
#pragma unroll
    for (int i = 0; i < 8; ++i) {
      const int sl = (14 + i) % RD;
#pragma unroll
      for (int t = 0; t < 2; ++t)
        rb[sl][t] = __builtin_bit_cast(bf16x8, __builtin_amdgcn_raw_buffer_load_b128(orsrc, (((2 * rt + t) * 32 + 8 * w + i) * 64 + lane) * 16, 0, 16));
    }
#pragma unroll
    for (int i = 0; i < 8; ++i) {
      const int sl = (14 + i) % RD;
#pragma unroll
      for (int c = 0; c < 2; ++c)
#pragma unroll
        for (int t = 0; t < 2; ++t) acc[c][t] = MFMA32(ra[sl][c], rb[sl][t], acc[c][t]);
    }
    char* red = lds + w * RED_TILE;
#pragma unroll
    for (int c = 0; c < 2; ++c)
#pragma unroll
      for (int t = 0; t < 2; ++t)
#pragma unroll
        for (int g = 0; g < 4; ++g) {
          f32x4 v = {acc[c][t][4 * g], acc[c][t][4 * g + 1], acc[c][t][4 * g + 2], acc[c][t][4 * g + 3]};
          *(f32x4*)(red + (32 * t + l31) * RED_STRIDE + (32 * c + 8 * g + 4 * h) * 4) = v;
        }
  }
  __syncthreads();
  {
    const int tok = tid >> 3, c8 = tid & 7;
    float v[8];
#pragma unroll
    for (int j = 0; j < 8; ++j) v[j] = 0.f;
#pragma unroll
    for (int p = 0; p < 4; ++p) {
      const f32x4 a = *(const f32x4*)(lds + p * RED_TILE + tok * RED_STRIDE + c8 * 32);
      const f32x4 c = *(const f32x4*)(lds + p * RED_TILE + tok * RED_STRIDE + c8 * 32 + 16);
#pragma unroll
      for (int j = 0; j < 4; ++j) { v[j] += a[j]; v[4 + j] += c[j]; }
    }
    *(bf16x8*)(out + (size_t)(64 * rt + tok) * 512 + 64 * jt + c8 * 8) = pack8(v);
  }
}

extern "C" void kernel_launch(void* const* d_in, const int* in_sizes, int n_in, void* d_out, int out_size, void* d_ws,
                              size_t ws_size, hipStream_t stream) {
  const unsigned short* x = (const unsigned short*)d_in[0];
  const unsigned short* wqkv = (const unsigned short*)d_in[1];
  const unsigned short* wo = (const unsigned short*)d_in[2];
  const unsigned short* w1 = (const unsigned short*)d_in[4];
  const unsigned short* b1 = (const unsigned short*)d_in[5];
  const unsigned short* w2 = (const unsigned short*)d_in[6];
  const unsigned short* gw = (const unsigned short*)d_in[7];
  k1_proj<<<256, 512, 0, stream>>>(x, wqkv, wo, w1, b1, w2, gw, (char*)d_ws);
  k2_attn_mlp<<<256, 512, 0, stream>>>((char*)d_ws, (unsigned short*)d_out);
}
```
